```python
import torch

def _softmax(x, axis=-1):
    return torch.softmax(x, dim=axis)
def _gelu(x):
    return torch.nn.functional.gelu(x, approximate="tanh")
def _silu(x):
    return torch.nn.functional.silu(x)
def _sigmoid(x):
    return torch.sigmoid(x)
def _layernorm(x, axis=-1, eps=1e-5):
    m = x.mean(dim=axis, keepdim=True)
    v = x.var(dim=axis, keepdim=True, unbiased=False)
    return (x - m) / torch.sqrt(v + eps)
def _scatter_add(tgt, idx, src, axis=0):
    return tgt.clone().index_add_(axis, idx, src)
def _segment_sum(src, seg, n):
    out = torch.zeros((n,) + src.shape[1:], dtype=src.dtype)
    return out.index_add_(0, seg, src)
def _pad(x, pw):
    flat = tuple(v for pair in reversed(pw) for v in pair)
    return torch.nn.functional.pad(x, flat)

kernel_name = 'concat_heads__gqa_block_cached__gqa_block_cached__near_ridge'

def setup_inputs():

    _w = (lambda a: (lambda t: t.to(torch.bfloat16) if t.is_floating_point() else t)(torch.as_tensor(a)))
    def _A_setup():
        B, Sq, D = 16, 32, 1024
        Hq, Hkv, Sk, Dh = 32, 4, 32, 128
        assert Hq % Hkv == 0, (Hq, Hkv)
        return {
            "x": _w((lambda *s: torch.randn(*s, dtype=torch.bfloat16) * 0.8)(B, Sq, D)),
            "k_cache": _w((lambda *s: torch.randn(*s, dtype=torch.bfloat16) * 0.8)(B, Hkv, Sk, Dh)),
            "v_cache": _w((lambda *s: torch.randn(*s, dtype=torch.bfloat16) * 0.05)(B, Hkv, Sk, Dh)),
            "wq": _w((lambda *s: torch.randn(*s, dtype=torch.bfloat16) * 0.8)(D, Hq * Dh) * (D ** -0.5)),
            "wo": _w((lambda *s: torch.randn(*s, dtype=torch.bfloat16) * 0.05)(Hq * Dh, D) * ((Hq * Dh) ** -0.5)),
        }
    _A_in = _A_setup()

    def _B_setup():
        B, Sq, D = 16, 32, 1024
        Hq, Hkv, Sk, Dh = 32, 4, 32, 128
        assert Hq % Hkv == 0, (Hq, Hkv)
        return {
            "x": _w((lambda *s: torch.randn(*s, dtype=torch.bfloat16) * 0.8)(B, Sq, D)),
            "k_cache": _w((lambda *s: torch.randn(*s, dtype=torch.bfloat16) * 0.8)(B, Hkv, Sk, Dh)),
            "v_cache": _w((lambda *s: torch.randn(*s, dtype=torch.bfloat16) * 0.05)(B, Hkv, Sk, Dh)),
            "wq": _w((lambda *s: torch.randn(*s, dtype=torch.bfloat16) * 0.8)(D, Hq * Dh) * (D ** -0.5)),
            "wo": _w((lambda *s: torch.randn(*s, dtype=torch.bfloat16) * 0.05)(Hq * Dh, D) * ((Hq * Dh) ** -0.5)),
        }
    _B_in = _B_setup()

    return {
        "A_x": _A_in["x"],
        "A_k_cache": _A_in["k_cache"],
        "A_v_cache": _A_in["v_cache"],
        "A_wq": _A_in["wq"],
        "A_wo": _A_in["wo"],
        "B_x": _B_in["x"],
        "B_k_cache": _B_in["k_cache"],
        "B_v_cache": _B_in["v_cache"],
        "B_wq": _B_in["wq"],
        "B_wo": _B_in["wo"],
    }

def reference(A_x, A_k_cache, A_v_cache, A_wq, A_wo, B_x, B_k_cache, B_v_cache, B_wq, B_wo):
    def _A_ref(x, k_cache, v_cache, wq, wo):
        B, Sq, D = x.shape
        Hq, Dh = 32, 128
        Hkv = k_cache.shape[1]
        rep = Hq // Hkv
        q = (lambda x, *d: x.permute(*d))((x @ wq).reshape(B, Sq, Hq, Dh), 0, 2, 1, 3)
        k = (lambda x, n, axis: torch.repeat_interleave(x, n, dim=axis))(k_cache, rep, axis=1)
        v = (lambda x, n, axis: torch.repeat_interleave(x, n, dim=axis))(v_cache, rep, axis=1)
        scores = torch.einsum("bhqd,bhkd->bhqk", q, k) * (Dh ** -0.5)
        p = _softmax(scores, axis=-1)
        o = torch.einsum("bhqk,bhkd->bhqd", p, v)
        return (lambda x, *d: x.permute(*d))(o, 0, 2, 1, 3).reshape(B, Sq, Hq * Dh) @ wo

    def _B_ref(x, k_cache, v_cache, wq, wo):
        B, Sq, D = x.shape
        Hq, Dh = 32, 128
        Hkv = k_cache.shape[1]
        rep = Hq // Hkv
        q = (lambda x, *d: x.permute(*d))((x @ wq).reshape(B, Sq, Hq, Dh), 0, 2, 1, 3)
        k = (lambda x, n, axis: torch.repeat_interleave(x, n, dim=axis))(k_cache, rep, axis=1)
        v = (lambda x, n, axis: torch.repeat_interleave(x, n, dim=axis))(v_cache, rep, axis=1)
        scores = torch.einsum("bhqd,bhkd->bhqk", q, k) * (Dh ** -0.5)
        p = _softmax(scores, axis=-1)
        o = torch.einsum("bhqk,bhkd->bhqd", p, v)
        return (lambda x, *d: x.permute(*d))(o, 0, 2, 1, 3).reshape(B, Sq, Hq * Dh) @ wo

    a = _A_ref(A_x, A_k_cache, A_v_cache, A_wq, A_wo)
    b = _B_ref(B_x, B_k_cache, B_v_cache, B_wq, B_wo)
    return torch.concatenate([a, b], axis=-1)
```

```cpp
#include <hip/hip_runtime.h>
#include <hip/hip_bf16.h>
#include <stdint.h>

typedef unsigned v4u __attribute__((ext_vector_type(4)));
typedef unsigned v2u __attribute__((ext_vector_type(2)));
typedef short s4 __attribute__((ext_vector_type(4)));
typedef short s8 __attribute__((ext_vector_type(8)));
typedef __bf16 bf8 __attribute__((ext_vector_type(8)));
typedef __bf16 bf2 __attribute__((ext_vector_type(2)));
typedef float f4v __attribute__((ext_vector_type(4)));
typedef float f2v __attribute__((ext_vector_type(2)));
#define LAS __attribute__((address_space(3)))
#define GAS __attribute__((address_space(1)))
#define DI __device__ __forceinline__
#define SB() __builtin_amdgcn_sched_barrier(0)

#define LT 7
#define STAG2 0

constexpr unsigned L_KP = 0;
constexpr unsigned L_RING = 32768;
constexpr unsigned L_V = 131072;
constexpr unsigned L_X = 147456;
constexpr unsigned LDS_BYTES = 163840;
constexpr size_t WS_P = 65536;

template <int N> struct IC { static constexpr int value = N; };
template <int I, int N, class F> DI void static_for(F&& f) { if constexpr (I < N) { f(IC<I>{}); static_for<I + 1, N>(f); } }

DI void dma4(unsigned v0, unsigned v1, unsigned v2, unsigned v3, const char* sb, unsigned lds) {
  unsigned keep;
  asm volatile("s_mov_b32 %0, m0\n\ts_mov_b32 m0, %6\n\ts_nop 0\n\t"
               "global_load_lds_dwordx4 %1, %5\n\tglobal_load_lds_dwordx4 %2, %5 offset:1024\n\t"
               "global_load_lds_dwordx4 %3, %5 offset:2048\n\tglobal_load_lds_dwordx4 %4, %5 offset:3072\n\ts_mov_b32 m0, %0"
               : "=&s"(keep) : "v"(v0), "v"(v1), "v"(v2), "v"(v3), "s"(sb), "s"(lds) : "memory");
}
DI void dma4_sc1(unsigned v0, const char* sb, unsigned lds) {
  unsigned keep;
  asm volatile("s_mov_b32 %0, m0\n\ts_mov_b32 m0, %3\n\ts_nop 0\n\t"
               "global_load_lds_dwordx4 %1, %2 sc1\n\tglobal_load_lds_dwordx4 %1, %2 offset:1024 sc1\n\t"
               "global_load_lds_dwordx4 %1, %2 offset:2048 sc1\n\tglobal_load_lds_dwordx4 %1, %2 offset:3072 sc1\n\ts_mov_b32 m0, %0"
               : "=&s"(keep) : "v"(v0), "s"(sb), "s"(lds) : "memory");
}
template <int N> DI void waitv() { asm volatile("s_waitcnt vmcnt(%0)" ::"i"(N) : "memory"); }
DI void bar() { asm volatile("s_barrier" ::: "memory"); }
DI void barl() { asm volatile("s_waitcnt lgkmcnt(0)\n\ts_barrier" ::: "memory"); }
DI s4 tr(unsigned a) { return __builtin_amdgcn_ds_read_tr16_b64_v4i16((LAS s4*)(size_t)a); }
DI s8 cat(s4 lo, s4 hi) { return __builtin_shufflevector(lo, hi, 0, 1, 2, 3, 4, 5, 6, 7); }
DI s8 rd16(unsigned a) { return *(LAS s8*)(size_t)a; }
DI s4 rd8(unsigned a) { return *(LAS s4*)(size_t)a; }
DI f4v mfma(s8 a, s8 b, f4v c) { return __builtin_amdgcn_mfma_f32_16x16x32_bf16(__builtin_bit_cast(bf8, a), __builtin_bit_cast(bf8, b), c, 0, 0, 0); }
#define MF "v_mfma_f32_16x16x32_bf16 %0, %1, %2, "
DI void mma(f4v& c, s8 a, s8 b) { asm volatile(MF "%0" : "+v"(c) : "v"(a), "v"(b)); }
DI void mma(f4v& c, s8 a, s8 b, s8 e0) { asm volatile(MF "%0" : "+v"(c) : "v"(a), "v"(b), "v"(e0)); }
DI void mma(f4v& c, s8 a, s8 b, s8 e0, s8 e1, s8 e2, s8 e3) { asm volatile(MF "%0" : "+v"(c) : "v"(a), "v"(b), "v"(e0), "v"(e1), "v"(e2), "v"(e3)); }
DI void mma0(f4v& c, s8 a, s8 b) { asm volatile(MF "0" : "=&v"(c) : "v"(a), "v"(b)); }
DI void mma0(f4v& c, s8 a, s8 b, s8 e0) { asm volatile(MF "0" : "=&v"(c) : "v"(a), "v"(b), "v"(e0)); }
DI void mfma_done(f4v& a, f4v& b, f4v& c, f4v& d) { asm volatile("s_nop 7\n\ts_nop 1" : "+v"(a), "+v"(b), "+v"(c), "+v"(d)); }
DI void pin4(f4v& a, f4v& b, f4v& c, f4v& d) { asm volatile("" : "+v"(a), "+v"(b), "+v"(c), "+v"(d)); }
DI unsigned pk(float a, float b) { f2v v = {a, b}; return __builtin_bit_cast(unsigned, __builtin_convertvector(v, bf2)); }
DI s8 pack8(f4v a, f4v b) { v4u r = {pk(a[0], a[1]), pk(a[2], a[3]), pk(b[0], b[1]), pk(b[2], b[3])}; return __builtin_bit_cast(s8, r); }
DI unsigned opaque(unsigned x) { asm volatile("" : "+v"(x)); return x; }
DI float xmax(float x) {
  v2u a = __builtin_amdgcn_permlane16_swap(__float_as_uint(x), __float_as_uint(x), false, false);
  x = fmaxf(__uint_as_float(a[0]), __uint_as_float(a[1]));
  v2u b = __builtin_amdgcn_permlane32_swap(__float_as_uint(x), __float_as_uint(x), false, false);
  return fmaxf(__uint_as_float(b[0]), __uint_as_float(b[1]));
}
DI float xsum(float x) {
  v2u a = __builtin_amdgcn_permlane16_swap(__float_as_uint(x), __float_as_uint(x), false, false);
  x = __uint_as_float(a[0]) + __uint_as_float(a[1]);
  v2u b = __builtin_amdgcn_permlane32_swap(__float_as_uint(x), __float_as_uint(x), false, false);
  return __uint_as_float(b[0]) + __uint_as_float(b[1]);
}
constexpr unsigned slot_off(int n) { return (unsigned)(n % 3) * 32768u; }
constexpr int pv_exec(int h) { const int e = 8 * (LT + (h >> 2)) + 2 * (h & 3); return e > 4 * h + 6 ? e : 4 * h + 6; }
constexpr int pv_read(int h) { const int lim = (h >> 2) <= 5 ? 8 * (LT + (h >> 2) + 1) - 3 : 127; return pv_exec(h) - 2 < lim ? pv_exec(h) - 2 : lim; }
constexpr bool pv_ok() { for (int h = 2; h < 32; ++h) if (pv_read(h) < pv_exec(h - 2)) return false; return pv_exec(30) <= 127 && LT + 7 <= 15 && LT >= 3; }
static_assert(pv_ok(), "P schedule");

struct Args {
  const char* x[2]; const char* k[2]; const char* v[2]; const char* wq[2]; const char* wo[2];
  char* out; char* ws;
};

__global__ __launch_bounds__(512, 2) void gqa_fused_kernel(Args a) {
  extern __shared__ __attribute__((aligned(16))) char smem[];
  const int lane = threadIdx.x & 63;
  const int w = __builtin_amdgcn_readfirstlane(threadIdx.x >> 6);
  const int xcd = blockIdx.x & 7, idx = blockIdx.x >> 3;
  const int br = xcd >> 2, hg = xcd & 3, i8 = idx & 7, rb = idx >> 3;
  const int head = hg * 8 + i8;
  const int ct = hg * 4 + (i8 & 3), rp = rb * 2 + (i8 >> 2);
  const int g0 = STAG2 ? (rp & 3) : 0;
  char* const wsP = a.ws + WS_P + (size_t)br * (16 * 32 * 2048);
  GAS unsigned* const flags = (GAS unsigned*)a.ws + br * 512;

  if (w >= 4) {
    const int lw = w - 4;
    const char* const x = br ? a.x[1] : a.x[0];
    const char* const wq = br ? a.wq[1] : a.wq[0];
    const char* const kc = br ? a.k[1] : a.k[0];
    const char* const vc = br ? a.v[1] : a.v[0];
    const char* const wo = br ? a.wo[1] : a.wo[0];
    unsigned vo[8], vk[8], vw[8];
    const char* sb; size_t tstride;
    if (lw < 2) {
#pragma unroll
      for (int i = 0; i < 8; ++i) { const int row = lw * 64 + i * 8 + (lane >> 3); vo[i] = row * 2048 + (((lane & 7) ^ ((row >> 1) & 7)) << 4) + 4096 - (i & 3) * 1024; }
      sb = x + (size_t)rb * 128 * 2048 - 4096; tstride = 128;
    } else {
#pragma unroll
      for (int i = 0; i < 8; ++i) { const int k = (lw - 2) * 32 + i * 4 + (lane >> 4); const int f = ((k & 3) | (((k >> 3) & 1) << 2)) << 1;
        vo[i] = k * 8192 + (((lane & 15) ^ f) << 4) + 4096 - (i & 3) * 1024; }
      sb = wq + head * 256 - 4096; tstride = 64 * 8192;
    }
    auto issueT = [&](int t, unsigned slot) { const char* s = sb + t * tstride; const unsigned l = L_RING + slot + lw * 8192;
      dma4(vo[0], vo[1], vo[2], vo[3], s, l); dma4(vo[4], vo[5], vo[6], vo[7], s, l + 4096); };
    issueT(0, slot_off(0)); issueT(1, slot_off(1));
#pragma unroll
    for (int i = 0; i < 8; ++i) { const int row = i * 4 + (lane >> 4); vk[i] = row * 256 + (((lane & 15) ^ (row & 15)) << 4) + 4096 - (i & 3) * 1024; }
#pragma unroll
    for (int i = 0; i < 8; ++i) { const int f = ((lane >> 4) & 1) | ((i & 1) << 1);
      vw[i] = (lw * 64 + i * 8 + (lane >> 3)) * 2048 + (((lane & 7) ^ (f << 1)) << 4) + 4096 - (i & 3) * 1024; }
    const unsigned vv0 = (lw & 1) ? vk[4] : vk[0], vv1 = (lw & 1) ? vk[5] : vk[1], vv2 = (lw & 1) ? vk[6] : vk[2], vv3 = (lw & 1) ? vk[7] : vk[3];
    auto issueW = [&](int j) { const int hp = (j + g0 * 4) & 15; const char* s = wo + (size_t)hp * 256 * 2048 + ct * 128 - 4096;
      const unsigned l = L_RING + slot_off(16 + j) + lw * 8192;
      dma4(vw[0], vw[1], vw[2], vw[3], s, l); dma4(vw[4], vw[5], vw[6], vw[7], s, l + 4096); };
    auto issueV = [&](int g) { const int gg = (g + g0) & 3; const char* s = vc + (size_t)((rp * 2 + (lw >> 1)) * 4 + gg) * 8192 - 4096;
      dma4(vv0, vv1, vv2, vv3, s, L_V + lw * 4096); };
    auto issueP = [&](int m) { const int h4 = (m * 4 + g0 * 8) & 31; const char* s = wsP + (size_t)((rp * 2 + (lw >> 1)) * 32 + h4 + (lw & 1) * 2) * 2048;
      dma4_sc1(lane * 16, s, L_KP + (m & 1) * 16384 + lw * 4096); };
    static_for<0, 14>([&](auto n) { waitv<8>(); bar(); issueT(n.value + 2, slot_off(n.value + 2)); });
    { const char* s = kc + (size_t)((rb * 4 + lw) * 4 + hg) * 8192 - 4096;
      dma4(vk[0], vk[1], vk[2], vk[3], s, L_KP + lw * 8192); dma4(vk[4], vk[5], vk[6], vk[7], s, L_KP + lw * 8192 + 4096); }
    waitv<16>(); bar(); issueV(0); issueW(0);
    waitv<20>(); bar(); issueW(1);
    waitv<20>(); bar(); issueW(2);
    bar();
    waitv<16>(); bar();
    unsigned pollv = 0;
    GAS unsigned* const myflag = flags + rp * 64 + lane;
    static_for<1, 16>([&](auto jj) {
      constexpr int j = jj.value;
      constexpr bool wprev = (j + 1 <= 15);
      if constexpr (wprev) waitv<8>(); else waitv<0>();
      bar();
      if constexpr (j == LT - 2) asm volatile("global_load_dword %0, %1, off sc1" : "=v"(pollv) : "v"(myflag) : "memory");
      if constexpr (j % 4 == 1 && j <= 9) issueV(j / 4 + 1);
      if constexpr (j == LT - 1) {
        asm volatile("" : "+v"(pollv));
        if (__builtin_amdgcn_ballot_w64(pollv != 1u) == 0) { issueP(0); issueW(j + 2); }
        else {
          issueW(j + 2);
          for (unsigned spins = 0; spins < (1u << 20); ++spins) {
            const unsigned f = __hip_atomic_load(myflag, __ATOMIC_RELAXED, __HIP_MEMORY_SCOPE_AGENT);
            if (__builtin_amdgcn_ballot_w64(f != 1u) == 0) break;
            __builtin_amdgcn_s_sleep(2);
          }
          asm volatile("" ::: "memory");
          issueP(0); waitv<0>();
        }
      } else {
        constexpr int m = j + 1 - LT;
        if constexpr (m >= 0 && m <= 7) issueP(m);
        if constexpr (j + 2 <= 15) issueW(j + 2);
      }
    });
  } else {
    __builtin_amdgcn_s_setprio(1);
    const int g = lane >> 4, q4 = (lane & 15) >> 2, p = lane & 3, r = lane & 15;
    const int rh = w >> 1, dh = w & 1;
    s8 Pfrag[2];
    {
      unsigned aA[2][4], aB[2][2][2];
#pragma unroll
      for (int c = 0; c < 4; ++c) { aA[0][c] = opaque(L_RING + 16384 + (8 * g + q4) * 256 + (((dh * 4 + c) ^ (q4 | ((g & 1) << 2))) << 5) + p * 8); aA[1][c] = opaque(aA[0][c] + 65536); }
#pragma unroll
      for (int kk = 0; kk < 2; ++kk)
#pragma unroll
        for (int o = 0; o < 2; ++o) { const int row = rh * 64 + ((o ^ dh ^ 1) << 5) + r;
          aB[0][kk][o] = opaque(L_RING + row * 128 + (((4 * kk + g) ^ ((row >> 1) & 7)) << 4)); aB[1][kk][o] = opaque(aB[0][kk][o] + 65536); }
      f4v acc[4][4];
      s4 Al[2][4], Ah[2][4]; s8 B[2][4];
      auto step = [&](auto first, auto bufc, auto rdn, auto tile, auto kkc) {
        constexpr int buf = bufc.value, nb = buf ^ 1, kk = kkc.value;
        constexpr unsigned so = slot_off(tile.value); constexpr int hi = so >= 65536; constexpr unsigned o = so - hi * 65536;
        static_for<0, 16>([&](auto mm) {
          constexpr int m = mm.value, c = m >> 2, b = m & 3;
          SB();
          if constexpr (first.value) mma0(acc[c][b], cat(Al[buf][c], Ah[buf][c]), B[buf][b]);
          else if constexpr (m == 0) mma(acc[c][b], cat(Al[buf][c], Ah[buf][c]), B[buf][b], B[buf][1], B[buf][2], B[buf][3], cat(Al[buf][1], Ah[buf][1]));
          else if constexpr (m == 8) mma(acc[c][b], cat(Al[buf][c], Ah[buf][c]), B[buf][b], cat(Al[buf][3], Ah[buf][3]));
          else mma(acc[c][b], cat(Al[buf][c], Ah[buf][c]), B[buf][b]);
          SB();
          if constexpr (rdn.value) {
            if constexpr (m == 0) B[nb][0] = rd16(aB[hi][kk][0] + o);
            if constexpr (m == 1) Al[nb][0] = tr(aA[hi][0] + o + kk * 8192);
            if constexpr (m == 2) Ah[nb][0] = tr(aA[hi][0] + o + kk * 8192 + 1024);
            if constexpr (m >= 3 && m <= 5) B[nb][m - 2] = rd16(aB[hi][kk][(m - 2) >> 1] + o + ((m - 2) & 1) * 2048);
            if constexpr (m >= 6 && m < 12) { constexpr int cc = 1 + (m - 6) / 2;
              if constexpr (m & 1) Ah[nb][cc] = tr(aA[hi][cc] + o + kk * 8192 + 1024); else Al[nb][cc] = tr(aA[hi][cc] + o + kk * 8192); }
          }
        });
        SB();
      };
      bar();
#pragma unroll
      for (int c = 0; c < 4; ++c) { Al[0][c] = tr(aA[0][c]); Ah[0][c] = tr(aA[0][c] + 1024); B[0][c] = rd16(aB[0][0][c >> 1] + (c & 1) * 2048); }
      static_for<0, 16>([&](auto t) {
        step(IC<(t.value == 0)>{}, IC<0>{}, IC<1>{}, t, IC<1>{});
        barl();
        step(IC<0>{}, IC<1>{}, IC<(t.value < 15)>{}, IC<t.value + 1>{}, IC<0>{});
      });
      s8 Kf[2][2][2];
#pragma unroll
      for (int bl = 0; bl < 2; ++bl)
#pragma unroll
        for (int kb = 0; kb < 2; ++kb)
#pragma unroll
          for (int s = 0; s < 2; ++s) {
            const unsigned rowb = L_KP + ((rh * 2 + (bl ^ dh ^ 1)) * 32 + kb * 16 + r) * 256 + (g & 1) * 8;
            const int ch = dh * 8 + s * 4 + (g >> 1);
            Kf[bl][kb][s] = cat(rd8(rowb + ((ch ^ r) << 4)), rd8(rowb + (((ch + 2) ^ r) << 4)));
          }
      SB();
      mfma_done(acc[3][0], acc[3][1], acc[3][2], acc[3][3]);
      pin4(acc[0][0], acc[0][1], acc[0][2], acc[0][3]); pin4(acc[1][0], acc[1][1], acc[1][2], acc[1][3]); pin4(acc[2][0], acc[2][1], acc[2][2], acc[2][3]);
      f4v St[2][2][2];
#pragma unroll
      for (int o = 0; o < 2; ++o) {
#pragma unroll
        for (int qb = 0; qb < 2; ++qb) {
          const s8 q0 = pack8(acc[0][2 * o + qb], acc[1][2 * o + qb]), q1 = pack8(acc[2][2 * o + qb], acc[3][2 * o + qb]);
          const f4v z = {0.f, 0.f, 0.f, 0.f};
          St[o][0][qb] = mfma(Kf[o][0][1], q1, mfma(Kf[o][0][0], q0, z));
          St[o][1][qb] = mfma(Kf[o][1][1], q1, mfma(Kf[o][1][0], q0, z));
        }
        if (o == 0) {
#pragma unroll
          for (int t = 0; t < 4; ++t) *(LAS f4v*)(size_t)(L_X + w * 4096 + t * 1024 + lane * 16) = St[0][t >> 1][t & 1];
        }
      }
      barl();
#pragma unroll
      for (int t = 0; t < 4; ++t) St[1][t >> 1][t & 1] += *(LAS f4v*)(size_t)(L_X + (w ^ 1) * 4096 + t * 1024 + lane * 16);
      const float cs = 0.08838834764831845f * 1.4426950408889634f;
#pragma unroll
      for (int qb = 0; qb < 2; ++qb) {
        f4v u = St[1][0][qb], v = St[1][1][qb];
        const float mx = xmax(fmaxf(fmaxf(fmaxf(u[0], u[1]), fmaxf(u[2], u[3])), fmaxf(fmaxf(v[0], v[1]), fmaxf(v[2], v[3]))));
        const float mc = mx * cs;
#pragma unroll
        for (int i = 0; i < 4; ++i) { u[i] = __builtin_amdgcn_exp2f(__builtin_fmaf(u[i], cs, -mc)); v[i] = __builtin_amdgcn_exp2f(__builtin_fmaf(v[i], cs, -mc)); }
        const float rs = __builtin_amdgcn_rcpf(xsum(((u[0] + u[1]) + (u[2] + u[3])) + ((v[0] + v[1]) + (v[2] + v[3]))));
        Pfrag[qb] = pack8(u * rs, v * rs);
      }
    }
    const int pb = rb * 4 + w;
    {
      const __amdgpu_buffer_rsrc_t rs = __builtin_amdgcn_make_buffer_rsrc(wsP + (size_t)(pb * 32 + head) * 2048, 0, 2048, 0x00020000);
      __builtin_amdgcn_raw_buffer_store_b128(__builtin_bit_cast(v4u, Pfrag[0]), rs, lane * 16, 0, 16);
      __builtin_amdgcn_raw_buffer_store_b128(__builtin_bit_cast(v4u, Pfrag[1]), rs, lane * 16 + 1024, 0, 16);
    }
    const int b2 = w >> 1, c2 = w & 1;
    unsigned aW[2][2];
#pragma unroll
    for (int nb = 0; nb < 2; ++nb) { aW[0][nb] = opaque(L_RING + (8 * g + q4) * 128 + (((c2 * 2 + nb) ^ ((q4 >> 1) | ((g & 1) << 1))) << 5) + p * 8); aW[1][nb] = opaque(aW[0][nb] + 65536); }
    const unsigned aP = opaque(L_KP + b2 * 8192 + lane * 16);
    const unsigned aV = opaque(L_V + (b2 * 32 + r) * 256 + ((g ^ r) << 4));
    s8 Vf[2][4];
    s4 Wl[3][2], Wh[3][2];
    f4v X[2][2][2];
    unsigned Xb[32][2][4];
    s8 Pf[2][2];
    f4v out[2][2];
    auto rdW = [&](auto uu, auto piece) {
      constexpr int u = uu.value, tile = 16 + (u >> 3), pc = piece.value;
      constexpr unsigned so = slot_off(tile); constexpr int hi = so >= 65536;
      constexpr unsigned o = so - hi * 65536 + (u & 7) * 4096 + (pc & 1) * 512;
      if constexpr (pc & 1) Wh[u % 3][pc >> 1] = tr(aW[hi][pc >> 1] + o); else Wl[u % 3][pc >> 1] = tr(aW[hi][pc >> 1] + o);
    };
    auto rdV = [&](auto kb, auto s) { Vf[kb.value][s.value] = rd16((aV ^ (s.value << 6)) + kb.value * 4096); };
    auto rdP = [&](auto hh, auto qb) { constexpr int h = hh.value; Pf[h & 1][qb.value] = rd16(aP + ((h >> 2) & 1) * 16384 + (h & 3) * 2048 + qb.value * 1024); };
    auto cvt = [&](auto hh, auto ii) { constexpr int h = hh.value, i = ii.value, nb = i >> 2, kb = (i >> 1) & 1, e = (i & 1) * 2;
      Xb[h][nb][i & 3] = opaque(pk(X[h & 1][kb][nb][e], X[h & 1][kb][nb][e + 1])); };
    auto pvm = [&](auto hh, auto ii) { constexpr int h = hh.value, nb = ii.value >> 1, qb = ii.value & 1;
      v4u xa = {Xb[h][nb][0], Xb[h][nb][1], Xb[h][nb][2], Xb[h][nb][3]};
      if constexpr (h == 0) { if constexpr (ii.value == 0) mma0(out[nb][qb], __builtin_bit_cast(s8, xa), Pf[h & 1][qb], Pf[h & 1][1]); else mma0(out[nb][qb], __builtin_bit_cast(s8, xa), Pf[h & 1][qb]); }
      else { if constexpr (ii.value == 0) mma(out[nb][qb], __builtin_bit_cast(s8, xa), Pf[h & 1][qb], Pf[h & 1][1]); else mma(out[nb][qb], __builtin_bit_cast(s8, xa), Pf[h & 1][qb]); } };
    const __amdgpu_buffer_rsrc_t frs = __builtin_amdgcn_make_buffer_rsrc((void*)(flags + pb * 32 + head), 0, 4, 0x00020000);
    bar();
    static_for<0, 4>([&](auto s) { rdV(IC<0>{}, s); rdV(IC<1>{}, s); if constexpr (s.value < 2) static_for<0, 4>([&](auto pc) { rdW(s, pc); }); });
    static_for<0, 128>([&](auto uu) {
      constexpr int u = uu.value, h = u >> 2, s = u & 3, j = u >> 3;
      constexpr bool cross = ((u + 2) & 7) == 0 && u + 2 < 128;
      static_for<0, 4>([&](auto mm) {
        constexpr int m = mm.value, nb = m >> 1, kb = m & 1;
        SB();
        if constexpr (s == 0) {
          if constexpr (m == 0) mma0(X[h & 1][kb][nb], Vf[kb][s], cat(Wl[u % 3][nb], Wh[u % 3][nb]), cat(Wl[u % 3][1], Wh[u % 3][1]));
          else mma0(X[h & 1][kb][nb], Vf[kb][s], cat(Wl[u % 3][nb], Wh[u % 3][nb]));
        } else {
          if constexpr (m == 0) mma(X[h & 1][kb][nb], Vf[kb][s], cat(Wl[u % 3][nb], Wh[u % 3][nb]), cat(Wl[u % 3][1], Wh[u % 3][1]));
          else mma(X[h & 1][kb][nb], Vf[kb][s], cat(Wl[u % 3][nb], Wh[u % 3][nb]));
        }
        SB();
        if constexpr (cross) {
          if constexpr (m == 1) {
            if constexpr (j == 0) {
              waitv<0>();
              __builtin_amdgcn_raw_buffer_store_b32(1u, frs, lane * 4, 0, 16);
            }
            barl(); rdW(IC<u + 2>{}, IC<0>{});
          }
          if constexpr (m == 2) { rdW(IC<u + 2>{}, IC<1>{}); rdW(IC<u + 2>{}, IC<2>{}); }
          if constexpr (m == 3) rdW(IC<u + 2>{}, IC<3>{});
        } else if constexpr (u + 2 < 128) rdW(IC<u + 2>{}, mm);
        if constexpr (h >= 1) { constexpr int i = s * 4 + m - 2; if constexpr (i >= 0 && i < 8) cvt(IC<h - 1>{}, IC<i>{}); }
        if constexpr ((h & 7) == 7 && h < 31 && m >= 2) rdV(IC<kb>{}, IC<s>{});
      });
      static_for<0, 31>([&](auto hh) { if constexpr (pv_exec(hh.value) == u) static_for<0, 4>([&](auto ii) { SB(); pvm(hh, ii); SB(); }); });
      static_for<0, 32>([&](auto hh) { if constexpr (pv_read(hh.value) == u) { rdP(hh, IC<0>{}); rdP(hh, IC<1>{}); } });
    });
    SB();
    mfma_done(X[1][0][0], X[1][0][1], X[1][1][0], X[1][1][1]);
    static_for<0, 8>([&](auto ii) { cvt(IC<31>{}, ii); });
    SB(); asm volatile("s_nop 1"); SB();
    static_for<0, 4>([&](auto ii) { pvm(IC<31>{}, ii); });
    SB();
    mfma_done(out[0][0], out[0][1], out[1][0], out[1][1]);
    const size_t orow = (size_t)((rp * 2 + b2) * 32 + r) * 4096 + br * 2048 + ct * 128 + c2 * 64 + ((g & 1) * 16 + (g >> 1) * 8) * 2;
#pragma unroll
    for (int qb = 0; qb < 2; ++qb) {
      v2u s0 = __builtin_amdgcn_permlane16_swap(pk(out[0][qb][0], out[0][qb][1]), pk(out[1][qb][0], out[1][qb][1]), false, false);
      v2u s1 = __builtin_amdgcn_permlane16_swap(pk(out[0][qb][2], out[0][qb][3]), pk(out[1][qb][2], out[1][qb][3]), false, false);
      v4u o4 = {s0[0], s1[0], s0[1], s1[1]};
      *(GAS v4u*)(a.out + orow + (size_t)qb * 16 * 4096) = o4;
    }
  }
}

extern "C" void kernel_launch(void* const* d_in, const int* in_sizes, int n_in, void* d_out, int out_size, void* d_ws, size_t ws_size,
                              hipStream_t stream) {
  Args a;
  for (int b = 0; b < 2; ++b) {
    a.x[b] = (const char*)d_in[b * 5 + 0]; a.k[b] = (const char*)d_in[b * 5 + 1]; a.v[b] = (const char*)d_in[b * 5 + 2];
    a.wq[b] = (const char*)d_in[b * 5 + 3]; a.wo[b] = (const char*)d_in[b * 5 + 4];
  }
  a.out = (char*)d_out; a.ws = (char*)d_ws;
  (void)hipFuncSetAttribute((const void*)gqa_fused_kernel, hipFuncAttributeMaxDynamicSharedMemorySize, LDS_BYTES);
  (void)hipMemsetAsync(d_ws, 0, 4096, stream);
  gqa_fused_kernel<<<256, 512, LDS_BYTES, stream>>>(a);
}
```

```cpp
#include <hip/hip_runtime.h>
#include <hip/hip_bf16.h>
#include <stdint.h>

typedef unsigned v4u __attribute__((ext_vector_type(4)));
typedef unsigned v2u __attribute__((ext_vector_type(2)));
typedef short s4 __attribute__((ext_vector_type(4)));
typedef short s8 __attribute__((ext_vector_type(8)));
typedef __bf16 bf8 __attribute__((ext_vector_type(8)));
typedef __bf16 bf2 __attribute__((ext_vector_type(2)));
typedef float f4v __attribute__((ext_vector_type(4)));
typedef float f2v __attribute__((ext_vector_type(2)));
#define LAS __attribute__((address_space(3)))
#define GAS __attribute__((address_space(1)))
#define DI __device__ __forceinline__
#define SB() __builtin_amdgcn_sched_barrier(0)

#define LT 7
#define STAG2 0

constexpr unsigned L_KP = 0;
constexpr unsigned L_RING = 32768;
constexpr unsigned L_V = 131072;
constexpr unsigned L_X = 147456;
constexpr unsigned LDS_BYTES = 163840;
constexpr size_t WS_P = 65536;

template <int N> struct IC { static constexpr int value = N; };
template <int I, int N, class F> DI void static_for(F&& f) { if constexpr (I < N) { f(IC<I>{}); static_for<I + 1, N>(f); } }

DI void dma4(unsigned v0, unsigned v1, unsigned v2, unsigned v3, const char* sb, unsigned lds) {
  unsigned keep;
  asm volatile("s_mov_b32 %0, m0\n\ts_mov_b32 m0, %6\n\ts_nop 0\n\t"
               "global_load_lds_dwordx4 %1, %5\n\tglobal_load_lds_dwordx4 %2, %5 offset:1024\n\t"
               "global_load_lds_dwordx4 %3, %5 offset:2048\n\tglobal_load_lds_dwordx4 %4, %5 offset:3072\n\ts_mov_b32 m0, %0"
               : "=&s"(keep) : "v"(v0), "v"(v1), "v"(v2), "v"(v3), "s"(sb), "s"(lds) : "memory");
}
DI void dma4_sc1(unsigned v0, const char* sb, unsigned lds) {
  unsigned keep;
  asm volatile("s_mov_b32 %0, m0\n\ts_mov_b32 m0, %3\n\ts_nop 0\n\t"
               "global_load_lds_dwordx4 %1, %2 sc1\n\tglobal_load_lds_dwordx4 %1, %2 offset:1024 sc1\n\t"
               "global_load_lds_dwordx4 %1, %2 offset:2048 sc1\n\tglobal_load_lds_dwordx4 %1, %2 offset:3072 sc1\n\ts_mov_b32 m0, %0"
               : "=&s"(keep) : "v"(v0), "s"(sb), "s"(lds) : "memory");
}
template <int N> DI void waitv() { asm volatile("s_waitcnt vmcnt(%0)" ::"i"(N) : "memory"); }
DI void bar() { asm volatile("s_barrier" ::: "memory"); }
DI void barl() { asm volatile("s_waitcnt lgkmcnt(0)\n\ts_barrier" ::: "memory"); }
DI s4 tr(unsigned a) { return __builtin_amdgcn_ds_read_tr16_b64_v4i16((LAS s4*)(size_t)a); }
DI s8 cat(s4 lo, s4 hi) { return __builtin_shufflevector(lo, hi, 0, 1, 2, 3, 4, 5, 6, 7); }
DI s8 rd16(unsigned a) { return *(LAS s8*)(size_t)a; }
DI s4 rd8(unsigned a) { return *(LAS s4*)(size_t)a; }
DI f4v mfma(s8 a, s8 b, f4v c) { return __builtin_amdgcn_mfma_f32_16x16x32_bf16(__builtin_bit_cast(bf8, a), __builtin_bit_cast(bf8, b), c, 0, 0, 0); }
#define MF "v_mfma_f32_16x16x32_bf16 %0, %1, %2, "
DI void mma(f4v& c, s8 a, s8 b) { asm volatile(MF "%0" : "+v"(c) : "v"(a), "v"(b)); }
DI void mma(f4v& c, s8 a, s8 b, s8 e0) { asm volatile(MF "%0" : "+v"(c) : "v"(a), "v"(b), "v"(e0)); }
DI void mma(f4v& c, s8 a, s8 b, s8 e0, s8 e1, s8 e2, s8 e3) { asm volatile(MF "%0" : "+v"(c) : "v"(a), "v"(b), "v"(e0), "v"(e1), "v"(e2), "v"(e3)); }
DI void mma0(f4v& c, s8 a, s8 b) { asm volatile(MF "0" : "=&v"(c) : "v"(a), "v"(b)); }
DI void mma0(f4v& c, s8 a, s8 b, s8 e0) { asm volatile(MF "0" : "=&v"(c) : "v"(a), "v"(b), "v"(e0)); }
DI void mfma_done(f4v& a, f4v& b, f4v& c, f4v& d) { asm volatile("s_nop 7\n\ts_nop 1" : "+v"(a), "+v"(b), "+v"(c), "+v"(d)); }
DI void pin4(f4v& a, f4v& b, f4v& c, f4v& d) { asm volatile("" : "+v"(a), "+v"(b), "+v"(c), "+v"(d)); }
DI unsigned pk(float a, float b) { f2v v = {a, b}; return __builtin_bit_cast(unsigned, __builtin_convertvector(v, bf2)); }
DI s8 pack8(f4v a, f4v b) { v4u r = {pk(a[0], a[1]), pk(a[2], a[3]), pk(b[0], b[1]), pk(b[2], b[3])}; return __builtin_bit_cast(s8, r); }
DI unsigned opaque(unsigned x) { asm volatile("" : "+v"(x)); return x; }
DI float xmax(float x) {
  v2u a = __builtin_amdgcn_permlane16_swap(__float_as_uint(x), __float_as_uint(x), false, false);
  x = fmaxf(__uint_as_float(a[0]), __uint_as_float(a[1]));
  v2u b = __builtin_amdgcn_permlane32_swap(__float_as_uint(x), __float_as_uint(x), false, false);
  return fmaxf(__uint_as_float(b[0]), __uint_as_float(b[1]));
}
DI float xsum(float x) {
  v2u a = __builtin_amdgcn_permlane16_swap(__float_as_uint(x), __float_as_uint(x), false, false);
  x = __uint_as_float(a[0]) + __uint_as_float(a[1]);
  v2u b = __builtin_amdgcn_permlane32_swap(__float_as_uint(x), __float_as_uint(x), false, false);
  return __uint_as_float(b[0]) + __uint_as_float(b[1]);
}
constexpr unsigned slot_off(int n) { return (unsigned)(n % 3) * 32768u; }
constexpr int pv_exec(int h) { const int e = 8 * (LT + (h >> 2)) + 2 * (h & 3); return e > 4 * h + 6 ? e : 4 * h + 6; }
constexpr int pv_read(int h) { const int lim = (h >> 2) <= 5 ? 8 * (LT + (h >> 2) + 1) - 3 : 127; return pv_exec(h) - 2 < lim ? pv_exec(h) - 2 : lim; }
constexpr bool pv_ok() { for (int h = 2; h < 32; ++h) if (pv_read(h) < pv_exec(h - 2)) return false; return pv_exec(30) <= 127 && LT + 7 <= 15 && LT >= 3; }
static_assert(pv_ok(), "P schedule");

#define STAMP(i)

struct Args {
  const char* x[2]; const char* k[2]; const char* v[2]; const char* wq[2]; const char* wo[2];
  char* out; char* ws;
};

__global__ __launch_bounds__(512, 2) void gqa_fused_kernel(Args a) {
  extern __shared__ __attribute__((aligned(16))) char smem[];
  const int lane = threadIdx.x & 63;
  const int w = __builtin_amdgcn_readfirstlane(threadIdx.x >> 6);
  const int xcd = blockIdx.x & 7, idx = blockIdx.x >> 3;
  const int br = xcd >> 2, hg = xcd & 3, i8 = idx & 7, rb = idx >> 3;
  const int head = hg * 8 + i8;
  const int ct = hg * 4 + (i8 & 3), rp = rb * 2 + (i8 >> 2);
  const int g0 = STAG2 ? (rp & 3) : 0;
  char* const wsP = a.ws + WS_P + (size_t)br * (16 * 32 * 2048);
  GAS unsigned* const flags = (GAS unsigned*)a.ws + br * 512;

  if (w >= 4) {
    const int lw = w - 4;
    const char* const x = br ? a.x[1] : a.x[0];
    const char* const wq = br ? a.wq[1] : a.wq[0];
    const char* const kc = br ? a.k[1] : a.k[0];
    const char* const vc = br ? a.v[1] : a.v[0];
    const char* const wo = br ? a.wo[1] : a.wo[0];
    unsigned vo[8], vk[8], vw[8];
    const char* sb; size_t tstride;
    if (lw < 2) {
#pragma unroll
      for (int i = 0; i < 8; ++i) { const int row = lw * 64 + i * 8 + (lane >> 3); vo[i] = row * 2048 + (((lane & 7) ^ ((row >> 1) & 7)) << 4) + 4096 - (i & 3) * 1024; }
      sb = x + (size_t)rb * 128 * 2048 - 4096; tstride = 128;
    } else {
#pragma unroll
      for (int i = 0; i < 8; ++i) { const int k = (lw - 2) * 32 + i * 4 + (lane >> 4); const int f = ((k & 3) | (((k >> 3) & 1) << 2)) << 1;
        vo[i] = k * 8192 + (((lane & 15) ^ f) << 4) + 4096 - (i & 3) * 1024; }
      sb = wq + head * 256 - 4096; tstride = 64 * 8192;
    }
    auto issueT = [&](int t, unsigned slot) { const char* s = sb + t * tstride; const unsigned l = L_RING + slot + lw * 8192;
      dma4(vo[0], vo[1], vo[2], vo[3], s, l); dma4(vo[4], vo[5], vo[6], vo[7], s, l + 4096); };
    issueT(0, slot_off(0)); issueT(1, slot_off(1));
#pragma unroll
    for (int i = 0; i < 8; ++i) { const int row = i * 4 + (lane >> 4); vk[i] = row * 256 + (((lane & 15) ^ (row & 15)) << 4) + 4096 - (i & 3) * 1024; }
#pragma unroll
    for (int i = 0; i < 8; ++i) { const int f = ((lane >> 4) & 1) | ((i & 1) << 1);
      vw[i] = (lw * 64 + i * 8 + (lane >> 3)) * 2048 + (((lane & 7) ^ (f << 1)) << 4) + 4096 - (i & 3) * 1024; }
    const unsigned vv0 = (lw & 1) ? vk[4] : vk[0], vv1 = (lw & 1) ? vk[5] : vk[1], vv2 = (lw & 1) ? vk[6] : vk[2], vv3 = (lw & 1) ? vk[7] : vk[3];
    auto issueW = [&](int j) { const int hp = (j + g0 * 4) & 15; const char* s = wo + (size_t)hp * 256 * 2048 + ct * 128 - 4096;
      const unsigned l = L_RING + slot_off(16 + j) + lw * 8192;
      dma4(vw[0], vw[1], vw[2], vw[3], s, l); dma4(vw[4], vw[5], vw[6], vw[7], s, l + 4096); };
    auto issueV = [&](int g) { const int gg = (g + g0) & 3; const char* s = vc + (size_t)((rp * 2 + (lw >> 1)) * 4 + gg) * 8192 - 4096;
      dma4(vv0, vv1, vv2, vv3, s, L_V + lw * 4096); };
    auto issueP = [&](int m) { const int h4 = (m * 4 + g0 * 8) & 31; const char* s = wsP + (size_t)((rp * 2 + (lw >> 1)) * 32 + h4 + (lw & 1) * 2) * 2048;
      dma4_sc1(lane * 16, s, L_KP + (m & 1) * 16384 + lw * 4096); };
    static_for<0, 14>([&](auto n) { waitv<8>(); bar(); issueT(n.value + 2, slot_off(n.value + 2)); });
    { const char* s = kc + (size_t)((rb * 4 + lw) * 4 + hg) * 8192 - 4096;
      dma4(vk[0], vk[1], vk[2], vk[3], s, L_KP + lw * 8192); dma4(vk[4], vk[5], vk[6], vk[7], s, L_KP + lw * 8192 + 4096); }
    waitv<16>(); bar(); issueV(0); issueW(0);
    waitv<20>(); bar(); issueW(1);
    waitv<20>(); bar(); issueW(2);
    bar();
    waitv<16>(); bar();
    unsigned pollv = 0;
    GAS unsigned* const myflag = flags + rp * 64 + lane;
    static_for<1, 16>([&](auto jj) {
      constexpr int j = jj.value;
      constexpr bool wprev = (j + 1 <= 15);
      if constexpr (wprev) waitv<8>(); else waitv<0>();
      bar();
      if constexpr (j == LT - 2) asm volatile("global_load_dword %0, %1, off sc1" : "=v"(pollv) : "v"(myflag) : "memory");
      if constexpr (j % 4 == 1 && j <= 9) issueV(j / 4 + 1);
      if constexpr (j == LT - 1) {
        asm volatile("" : "+v"(pollv));
        if (__builtin_amdgcn_ballot_w64(pollv != 1u) == 0) { issueP(0); issueW(j + 2); }
        else {
          issueW(j + 2);
          for (unsigned spins = 0; spins < (1u << 20); ++spins) {
            const unsigned f = __hip_atomic_load(myflag, __ATOMIC_RELAXED, __HIP_MEMORY_SCOPE_AGENT);
            if (__builtin_amdgcn_ballot_w64(f != 1u) == 0) break;
            __builtin_amdgcn_s_sleep(2);
          }
          asm volatile("" ::: "memory");
          issueP(0); waitv<0>();
        }
      } else {
        constexpr int m = j + 1 - LT;
        if constexpr (m >= 0 && m <= 7) issueP(m);
        if constexpr (j + 2 <= 15) issueW(j + 2);
      }
    });
  } else {
    STAMP(0);
    __builtin_amdgcn_s_setprio(1);
    const int g = lane >> 4, q4 = (lane & 15) >> 2, p = lane & 3, r = lane & 15;
    const int rh = w >> 1, dh = w & 1;
    s8 Pfrag[2];
    {
      unsigned aA[2][4], aB[2][2][2];
#pragma unroll
      for (int c = 0; c < 4; ++c) { aA[0][c] = opaque(L_RING + 16384 + (8 * g + q4) * 256 + (((dh * 4 + c) ^ (q4 | ((g & 1) << 2))) << 5) + p * 8); aA[1][c] = opaque(aA[0][c] + 65536); }
#pragma unroll
      for (int kk = 0; kk < 2; ++kk)
#pragma unroll
        for (int o = 0; o < 2; ++o) { const int row = rh * 64 + ((o ^ dh ^ 1) << 5) + r;
          aB[0][kk][o] = opaque(L_RING + row * 128 + (((4 * kk + g) ^ ((row >> 1) & 7)) << 4)); aB[1][kk][o] = opaque(aB[0][kk][o] + 65536); }
      f4v acc[4][4];
      s4 Al[2][4], Ah[2][4]; s8 B[2][4];
      auto step = [&](auto first, auto bufc, auto rdn, auto tile, auto kkc) {
        constexpr int buf = bufc.value, nb = buf ^ 1, kk = kkc.value;
        constexpr unsigned so = slot_off(tile.value); constexpr int hi = so >= 65536; constexpr unsigned o = so - hi * 65536;
        static_for<0, 16>([&](auto mm) {
          constexpr int m = mm.value, c = m >> 2, b = m & 3;
          SB();
          if constexpr (first.value) mma0(acc[c][b], cat(Al[buf][c], Ah[buf][c]), B[buf][b]);
          else if constexpr (m == 0) mma(acc[c][b], cat(Al[buf][c], Ah[buf][c]), B[buf][b], B[buf][1], B[buf][2], B[buf][3], cat(Al[buf][1], Ah[buf][1]));
          else if constexpr (m == 8) mma(acc[c][b], cat(Al[buf][c], Ah[buf][c]), B[buf][b], cat(Al[buf][3], Ah[buf][3]));
          else mma(acc[c][b], cat(Al[buf][c], Ah[buf][c]), B[buf][b]);
          SB();
          if constexpr (rdn.value) {
            if constexpr (m == 0) B[nb][0] = rd16(aB[hi][kk][0] + o);
            if constexpr (m == 1) Al[nb][0] = tr(aA[hi][0] + o + kk * 8192);
            if constexpr (m == 2) Ah[nb][0] = tr(aA[hi][0] + o + kk * 8192 + 1024);
            if constexpr (m >= 3 && m <= 5) B[nb][m - 2] = rd16(aB[hi][kk][(m - 2) >> 1] + o + ((m - 2) & 1) * 2048);
            if constexpr (m >= 6 && m < 12) { constexpr int cc = 1 + (m - 6) / 2;
              if constexpr (m & 1) Ah[nb][cc] = tr(aA[hi][cc] + o + kk * 8192 + 1024); else Al[nb][cc] = tr(aA[hi][cc] + o + kk * 8192); }
          }
        });
        SB();
      };
      bar();
      STAMP(1);
#pragma unroll
      for (int c = 0; c < 4; ++c) { Al[0][c] = tr(aA[0][c]); Ah[0][c] = tr(aA[0][c] + 1024); B[0][c] = rd16(aB[0][0][c >> 1] + (c & 1) * 2048); }
      static_for<0, 16>([&](auto t) {
        step(IC<(t.value == 0)>{}, IC<0>{}, IC<1>{}, t, IC<1>{});
        barl();
        if constexpr (t.value == 7) STAMP(2);
        if constexpr (t.value == 15) STAMP(3);
        step(IC<0>{}, IC<1>{}, IC<(t.value < 15)>{}, IC<t.value + 1>{}, IC<0>{});
      });
      s8 Kf[2][2][2];
#pragma unroll
      for (int bl = 0; bl < 2; ++bl)
#pragma unroll
        for (int kb = 0; kb < 2; ++kb)
#pragma unroll
          for (int s = 0; s < 2; ++s) {
            const unsigned rowb = L_KP + ((rh * 2 + (bl ^ dh ^ 1)) * 32 + kb * 16 + r) * 256 + (g & 1) * 8;
            const int ch = dh * 8 + s * 4 + (g >> 1);
            Kf[bl][kb][s] = cat(rd8(rowb + ((ch ^ r) << 4)), rd8(rowb + (((ch + 2) ^ r) << 4)));
          }
      SB();
      mfma_done(acc[3][0], acc[3][1], acc[3][2], acc[3][3]);
      pin4(acc[0][0], acc[0][1], acc[0][2], acc[0][3]); pin4(acc[1][0], acc[1][1], acc[1][2], acc[1][3]); pin4(acc[2][0], acc[2][1], acc[2][2], acc[2][3]);
      f4v St[2][2][2];
#pragma unroll
      for (int o = 0; o < 2; ++o) {
#pragma unroll
        for (int qb = 0; qb < 2; ++qb) {
          const s8 q0 = pack8(acc[0][2 * o + qb], acc[1][2 * o + qb]), q1 = pack8(acc[2][2 * o + qb], acc[3][2 * o + qb]);
          const f4v z = {0.f, 0.f, 0.f, 0.f};
          St[o][0][qb] = mfma(Kf[o][0][1], q1, mfma(Kf[o][0][0], q0, z));
          St[o][1][qb] = mfma(Kf[o][1][1], q1, mfma(Kf[o][1][0], q0, z));
        }
        if (o == 0) {
#pragma unroll
          for (int t = 0; t < 4; ++t) *(LAS f4v*)(size_t)(L_X + w * 4096 + t * 1024 + lane * 16) = St[0][t >> 1][t & 1];
        }
      }
      barl();
      STAMP(4);
#pragma unroll
      for (int t = 0; t < 4; ++t) St[1][t >> 1][t & 1] += *(LAS f4v*)(size_t)(L_X + (w ^ 1) * 4096 + t * 1024 + lane * 16);
      const float cs = 0.08838834764831845f * 1.4426950408889634f;
#pragma unroll
      for (int qb = 0; qb < 2; ++qb) {
        f4v u = St[1][0][qb], v = St[1][1][qb];
        const float mx = xmax(fmaxf(fmaxf(fmaxf(u[0], u[1]), fmaxf(u[2], u[3])), fmaxf(fmaxf(v[0], v[1]), fmaxf(v[2], v[3]))));
        const float mc = mx * cs;
#pragma unroll
        for (int i = 0; i < 4; ++i) { u[i] = __builtin_amdgcn_exp2f(__builtin_fmaf(u[i], cs, -mc)); v[i] = __builtin_amdgcn_exp2f(__builtin_fmaf(v[i], cs, -mc)); }
        const float rs = __builtin_amdgcn_rcpf(xsum(((u[0] + u[1]) + (u[2] + u[3])) + ((v[0] + v[1]) + (v[2] + v[3]))));
        Pfrag[qb] = pack8(u * rs, v * rs);
      }
    }
    const int pb = rb * 4 + w;
    {
      const __amdgpu_buffer_rsrc_t rs = __builtin_amdgcn_make_buffer_rsrc(wsP + (size_t)(pb * 32 + head) * 2048, 0, 2048, 0x00020000);
      __builtin_amdgcn_raw_buffer_store_b128(__builtin_bit_cast(v4u, Pfrag[0]), rs, lane * 16, 0, 16);
      __builtin_amdgcn_raw_buffer_store_b128(__builtin_bit_cast(v4u, Pfrag[1]), rs, lane * 16 + 1024, 0, 16);
    }
    const int b2 = w >> 1, c2 = w & 1;
    unsigned aW[2][2];
#pragma unroll
    for (int nb = 0; nb < 2; ++nb) { aW[0][nb] = opaque(L_RING + (8 * g + q4) * 128 + (((c2 * 2 + nb) ^ ((q4 >> 1) | ((g & 1) << 1))) << 5) + p * 8); aW[1][nb] = opaque(aW[0][nb] + 65536); }
    const unsigned aP = opaque(L_KP + b2 * 8192 + lane * 16);
    const unsigned aV = opaque(L_V + (b2 * 32 + r) * 256 + ((g ^ r) << 4));
    s8 Vf[2][4];
    s4 Wl[3][2], Wh[3][2];
    f4v X[2][2][2];
    unsigned Xb[32][2][4];
    s8 Pf[2][2];
    f4v out[2][2];
    auto rdW = [&](auto uu, auto piece) {
      constexpr int u = uu.value, tile = 16 + (u >> 3), pc = piece.value;
      constexpr unsigned so = slot_off(tile); constexpr int hi = so >= 65536;
      constexpr unsigned o = so - hi * 65536 + (u & 7) * 4096 + (pc & 1) * 512;
      if constexpr (pc & 1) Wh[u % 3][pc >> 1] = tr(aW[hi][pc >> 1] + o); else Wl[u % 3][pc >> 1] = tr(aW[hi][pc >> 1] + o);
    };
    auto rdV = [&](auto kb, auto s) { Vf[kb.value][s.value] = rd16((aV ^ (s.value << 6)) + kb.value * 4096); };
    auto rdP = [&](auto hh, auto qb) { constexpr int h = hh.value; Pf[h & 1][qb.value] = rd16(aP + ((h >> 2) & 1) * 16384 + (h & 3) * 2048 + qb.value * 1024); };
    auto cvt = [&](auto hh, auto ii) { constexpr int h = hh.value, i = ii.value, nb = i >> 2, kb = (i >> 1) & 1, e = (i & 1) * 2;
      Xb[h][nb][i & 3] = opaque(pk(X[h & 1][kb][nb][e], X[h & 1][kb][nb][e + 1])); };
    auto pvm = [&](auto hh, auto ii) { constexpr int h = hh.value, nb = ii.value >> 1, qb = ii.value & 1;
      v4u xa = {Xb[h][nb][0], Xb[h][nb][1], Xb[h][nb][2], Xb[h][nb][3]};
      if constexpr (h == 0) { if constexpr (ii.value == 0) mma0(out[nb][qb], __builtin_bit_cast(s8, xa), Pf[h & 1][qb], Pf[h & 1][1]); else mma0(out[nb][qb], __builtin_bit_cast(s8, xa), Pf[h & 1][qb]); }
      else { if constexpr (ii.value == 0) mma(out[nb][qb], __builtin_bit_cast(s8, xa), Pf[h & 1][qb], Pf[h & 1][1]); else mma(out[nb][qb], __builtin_bit_cast(s8, xa), Pf[h & 1][qb]); } };
    const __amdgpu_buffer_rsrc_t frs = __builtin_amdgcn_make_buffer_rsrc((void*)(flags + pb * 32 + head), 0, 4, 0x00020000);
    STAMP(5);
    bar();
    STAMP(6);
    static_for<0, 4>([&](auto s) { rdV(IC<0>{}, s); rdV(IC<1>{}, s); if constexpr (s.value < 2) static_for<0, 4>([&](auto pc) { rdW(s, pc); }); });
    static_for<0, 128>([&](auto uu) {
      constexpr int u = uu.value, h = u >> 2, s = u & 3, j = u >> 3;
      constexpr bool cross = ((u + 2) & 7) == 0 && u + 2 < 128;
      static_for<0, 4>([&](auto mm) {
        constexpr int m = mm.value, nb = m >> 1, kb = m & 1;
        SB();
        if constexpr (s == 0) {
          if constexpr (m == 0) mma0(X[h & 1][kb][nb], Vf[kb][s], cat(Wl[u % 3][nb], Wh[u % 3][nb]), cat(Wl[u % 3][1], Wh[u % 3][1]));
          else mma0(X[h & 1][kb][nb], Vf[kb][s], cat(Wl[u % 3][nb], Wh[u % 3][nb]));
        } else {
          if constexpr (m == 0) mma(X[h & 1][kb][nb], Vf[kb][s], cat(Wl[u % 3][nb], Wh[u % 3][nb]), cat(Wl[u % 3][1], Wh[u % 3][1]));
          else mma(X[h & 1][kb][nb], Vf[kb][s], cat(Wl[u % 3][nb], Wh[u % 3][nb]));
        }
        SB();
        if constexpr (cross) {
          if constexpr (m == 1) {
            if constexpr (j == 0) {
              waitv<0>();
              __builtin_amdgcn_raw_buffer_store_b32(1u, frs, lane * 4, 0, 16);
            }
            barl();
            if constexpr (j == 0) STAMP(7);
            if constexpr (j == LT - 2) STAMP(8);
            if constexpr (j == LT - 1) STAMP(9);
            if constexpr (j == 14) STAMP(10);
            rdW(IC<u + 2>{}, IC<0>{});
          }
          if constexpr (m == 2) { rdW(IC<u + 2>{}, IC<1>{}); rdW(IC<u + 2>{}, IC<2>{}); }
          if constexpr (m == 3) rdW(IC<u + 2>{}, IC<3>{});
        } else if constexpr (u + 2 < 128) rdW(IC<u + 2>{}, mm);
        if constexpr (h >= 1) { constexpr int i = s * 4 + m - 2; if constexpr (i >= 0 && i < 8) cvt(IC<h - 1>{}, IC<i>{}); }
        if constexpr ((h & 7) == 7 && h < 31 && m >= 2) rdV(IC<kb>{}, IC<s>{});
      });
      static_for<0, 31>([&](auto hh) { if constexpr (pv_exec(hh.value) == u) static_for<0, 4>([&](auto ii) { SB(); pvm(hh, ii); SB(); }); });
      static_for<0, 32>([&](auto hh) { if constexpr (pv_read(hh.value) == u) { rdP(hh, IC<0>{}); rdP(hh, IC<1>{}); } });
    });
    SB();
    mfma_done(X[1][0][0], X[1][0][1], X[1][1][0], X[1][1][1]);
    static_for<0, 8>([&](auto ii) { cvt(IC<31>{}, ii); });
    SB(); asm volatile("s_nop 1"); SB();
    static_for<0, 4>([&](auto ii) { pvm(IC<31>{}, ii); });
    SB();
    mfma_done(out[0][0], out[0][1], out[1][0], out[1][1]);
    const size_t orow = (size_t)((rp * 2 + b2) * 32 + r) * 4096 + br * 2048 + ct * 128 + c2 * 64 + ((g & 1) * 16 + (g >> 1) * 8) * 2;
#pragma unroll
    for (int qb = 0; qb < 2; ++qb) {
      v2u s0 = __builtin_amdgcn_permlane16_swap(pk(out[0][qb][0], out[0][qb][1]), pk(out[1][qb][0], out[1][qb][1]), false, false);
      v2u s1 = __builtin_amdgcn_permlane16_swap(pk(out[0][qb][2], out[0][qb][3]), pk(out[1][qb][2], out[1][qb][3]), false, false);
      v4u o4 = {s0[0], s1[0], s0[1], s1[1]};
      *(GAS v4u*)(a.out + orow + (size_t)qb * 16 * 4096) = o4;
    }
  }
}

extern "C" void kernel_launch(void* const* d_in, const int* in_sizes, int n_in, void* d_out, int out_size, void* d_ws, size_t ws_size,
                              hipStream_t stream) {
  Args a;
  for (int b = 0; b < 2; ++b) {
    a.x[b] = (const char*)d_in[b * 5 + 0]; a.k[b] = (const char*)d_in[b * 5 + 1]; a.v[b] = (const char*)d_in[b * 5 + 2];
    a.wq[b] = (const char*)d_in[b * 5 + 3]; a.wo[b] = (const char*)d_in[b * 5 + 4];
  }
  a.out = (char*)d_out; a.ws = (char*)d_ws;
  (void)hipFuncSetAttribute((const void*)gqa_fused_kernel, hipFuncAttributeMaxDynamicSharedMemorySize, LDS_BYTES);

  gqa_fused_kernel<<<256, 512, LDS_BYTES, stream>>>(a);
}
```

```cpp
#include <hip/hip_runtime.h>
#include <hip/hip_bf16.h>
#include <stdint.h>

typedef unsigned v4u __attribute__((ext_vector_type(4)));
typedef unsigned v2u __attribute__((ext_vector_type(2)));
typedef short s4 __attribute__((ext_vector_type(4)));
typedef short s8 __attribute__((ext_vector_type(8)));
typedef __bf16 bf8 __attribute__((ext_vector_type(8)));
typedef __bf16 bf2 __attribute__((ext_vector_type(2)));
typedef float f4v __attribute__((ext_vector_type(4)));
typedef float f2v __attribute__((ext_vector_type(2)));
#define LAS __attribute__((address_space(3)))
#define GAS __attribute__((address_space(1)))
#define DI __device__ __forceinline__
#define SB() __builtin_amdgcn_sched_barrier(0)

#define LT 7
#define STAG1 3
#define STAG2 1

constexpr unsigned L_KP = 0;
constexpr unsigned L_RING = 32768;
constexpr unsigned L_V = 131072;
constexpr unsigned L_M = 147456;
constexpr unsigned L_X = L_RING;
constexpr unsigned LDS_BYTES = 163840;
constexpr size_t WS_N = 0, WS_I = 4096, WS_P = 65536;

template <int N> struct IC { static constexpr int value = N; };
template <int I, int N, class F> DI void static_for(F&& f) { if constexpr (I < N) { f(IC<I>{}); static_for<I + 1, N>(f); } }

DI void dma4(unsigned v0, unsigned v1, unsigned v2, unsigned v3, const char* sb, unsigned lds) {
  unsigned keep;
  asm volatile("s_mov_b32 %0, m0\n\ts_mov_b32 m0, %6\n\ts_nop 0\n\t"
               "global_load_lds_dwordx4 %1, %5\n\tglobal_load_lds_dwordx4 %2, %5 offset:1024\n\t"
               "global_load_lds_dwordx4 %3, %5 offset:2048\n\tglobal_load_lds_dwordx4 %4, %5 offset:3072\n\ts_mov_b32 m0, %0"
               : "=&s"(keep) : "v"(v0), "v"(v1), "v"(v2), "v"(v3), "s"(sb), "s"(lds) : "memory");
}
DI void dma4_sc1(unsigned v0, const char* sb, unsigned lds) {
  unsigned keep;
  asm volatile("s_mov_b32 %0, m0\n\ts_mov_b32 m0, %3\n\ts_nop 0\n\t"
               "global_load_lds_dwordx4 %1, %2 sc1\n\tglobal_load_lds_dwordx4 %1, %2 offset:1024 sc1\n\t"
               "global_load_lds_dwordx4 %1, %2 offset:2048 sc1\n\tglobal_load_lds_dwordx4 %1, %2 offset:3072 sc1\n\ts_mov_b32 m0, %0"
               : "=&s"(keep) : "v"(v0), "s"(sb), "s"(lds) : "memory");
}
template <int N> DI void waitv() { asm volatile("s_waitcnt vmcnt(%0)" ::"i"(N) : "memory"); }
DI void bar() { asm volatile("s_barrier" ::: "memory"); }
DI void barl() { asm volatile("s_waitcnt lgkmcnt(0)\n\ts_barrier" ::: "memory"); }
DI s4 tr(unsigned a) { return __builtin_amdgcn_ds_read_tr16_b64_v4i16((LAS s4*)(size_t)a); }
DI s8 cat(s4 lo, s4 hi) { return __builtin_shufflevector(lo, hi, 0, 1, 2, 3, 4, 5, 6, 7); }
DI s8 rd16(unsigned a) { return *(LAS s8*)(size_t)a; }
DI s4 rd8(unsigned a) { return *(LAS s4*)(size_t)a; }
DI f4v mfma(s8 a, s8 b, f4v c) { return __builtin_amdgcn_mfma_f32_16x16x32_bf16(__builtin_bit_cast(bf8, a), __builtin_bit_cast(bf8, b), c, 0, 0, 0); }
#define MF "v_mfma_f32_16x16x32_bf16 %0, %1, %2, "
DI void mma(f4v& c, s8 a, s8 b) { asm volatile(MF "%0" : "+v"(c) : "v"(a), "v"(b)); }
DI void mma(f4v& c, s8 a, s8 b, s8 e0) { asm volatile(MF "%0" : "+v"(c) : "v"(a), "v"(b), "v"(e0)); }
DI void mma(f4v& c, s8 a, s8 b, s8 e0, s8 e1, s8 e2, s8 e3) { asm volatile(MF "%0" : "+v"(c) : "v"(a), "v"(b), "v"(e0), "v"(e1), "v"(e2), "v"(e3)); }
DI void mma0(f4v& c, s8 a, s8 b) { asm volatile(MF "0" : "=&v"(c) : "v"(a), "v"(b)); }
DI void mma0(f4v& c, s8 a, s8 b, s8 e0) { asm volatile(MF "0" : "=&v"(c) : "v"(a), "v"(b), "v"(e0)); }
DI void mfma_done(f4v& a, f4v& b, f4v& c, f4v& d) { asm volatile("s_nop 7\n\ts_nop 1" : "+v"(a), "+v"(b), "+v"(c), "+v"(d)); }
DI void pin4(f4v& a, f4v& b, f4v& c, f4v& d) { asm volatile("" : "+v"(a), "+v"(b), "+v"(c), "+v"(d)); }
DI unsigned pk(float a, float b) { f2v v = {a, b}; return __builtin_bit_cast(unsigned, __builtin_convertvector(v, bf2)); }
DI s8 pack8(f4v a, f4v b) { v4u r = {pk(a[0], a[1]), pk(a[2], a[3]), pk(b[0], b[1]), pk(b[2], b[3])}; return __builtin_bit_cast(s8, r); }
DI unsigned opaque(unsigned x) { asm volatile("" : "+v"(x)); return x; }
DI float xmax(float x) {
  v2u a = __builtin_amdgcn_permlane16_swap(__float_as_uint(x), __float_as_uint(x), false, false);
  x = fmaxf(__uint_as_float(a[0]), __uint_as_float(a[1]));
  v2u b = __builtin_amdgcn_permlane32_swap(__float_as_uint(x), __float_as_uint(x), false, false);
  return fmaxf(__uint_as_float(b[0]), __uint_as_float(b[1]));
}
DI float xsum(float x) {
  v2u a = __builtin_amdgcn_permlane16_swap(__float_as_uint(x), __float_as_uint(x), false, false);
  x = __uint_as_float(a[0]) + __uint_as_float(a[1]);
  v2u b = __builtin_amdgcn_permlane32_swap(__float_as_uint(x), __float_as_uint(x), false, false);
  return __uint_as_float(b[0]) + __uint_as_float(b[1]);
}
constexpr unsigned slot_off(int n) { return (unsigned)(n % 3) * 32768u; }
constexpr int pv_exec(int h) { const int e = 8 * (LT + (h >> 2)) + 2 * (h & 3); return e > 4 * h + 6 ? e : 4 * h + 6; }
constexpr int pv_read(int h) { const int lim = (h >> 2) <= 5 ? 8 * (LT + (h >> 2) + 1) - 3 : 127; return pv_exec(h) - 2 < lim ? pv_exec(h) - 2 : lim; }
constexpr bool pv_ok() { for (int h = 2; h < 32; ++h) if (pv_read(h) < pv_exec(h - 2)) return false; return pv_exec(30) <= 127 && LT + 7 <= 15 && LT >= 3; }
static_assert(pv_ok(), "P schedule");
constexpr int exec_at(int u) { for (int h = 0; h < 31; ++h) if (pv_exec(h) == u) return h; return -1; }
constexpr int read_at(int u) { for (int h = 0; h < 32; ++h) if (pv_read(h) == u) return h; return -1; }
constexpr bool pv_unique() { for (int h = 1; h < 32; ++h) if (pv_exec(h) == pv_exec(h - 1) || pv_read(h) == pv_read(h - 1)) return false; return true; }
static_assert(pv_unique(), "one head per s-step");

#define STAMP(i)

struct Args {
  const char* x[2]; const char* k[2]; const char* v[2]; const char* wq[2]; const char* wo[2];
  char* out; char* ws;
};

__global__ __launch_bounds__(512, 2) void gqa_fused_kernel(Args a) {
#if __HIP_DEVICE_COMPILE__
  const int lane = threadIdx.x & 63;
  const int w = __builtin_amdgcn_readfirstlane(threadIdx.x >> 6);
  const int xcd = blockIdx.x & 7, idx = blockIdx.x >> 3;
  const int br = xcd >> 2, hg = xcd & 3, i8 = idx & 7, rb = idx >> 3;
  const int head = hg * 8 + i8;
  const int ct = hg * 4 + (i8 & 3), rp = rb * 2 + (i8 >> 2);
  const int ord = STAG2 ? ((rp + 2 * ct) & 7) : 0, g0 = ord >> 1, par = ord & 1;
  const int kt0 = STAG1 == 1 ? 4 * rb + 2 * i8 : STAG1 == 2 ? 8 * (rb & 1) + 4 * (i8 & 3) : STAG1 == 3 ? 8 * ((rb ^ i8) & 1) : 0;
  char* const wsP = a.ws + WS_P + (size_t)br * (16 * 32 * 2048);

  if (w >= 4) {
    const int lw = w - 4;
    const char* const x = br ? a.x[1] : a.x[0];
    const char* const wq = br ? a.wq[1] : a.wq[0];
    const char* const kc = br ? a.k[1] : a.k[0];
    const char* const vc = br ? a.v[1] : a.v[0];
    const char* const wo = br ? a.wo[1] : a.wo[0];
    unsigned vo[8], vk[8], vw[8];
    const char* sb; size_t tstride;
    if (lw < 2) {
#pragma unroll
      for (int i = 0; i < 8; ++i) { const int row = lw * 64 + i * 8 + (lane >> 3); vo[i] = row * 2048 + (((lane & 7) ^ ((row >> 1) & 7)) << 4) + 4096 - (i & 3) * 1024; }
      sb = x + (size_t)rb * 128 * 2048 - 4096; tstride = 128;
    } else {
#pragma unroll
      for (int i = 0; i < 8; ++i) { const int k = (lw - 2) * 32 + i * 4 + (lane >> 4); const int f = ((k & 3) | (((k >> 3) & 1) << 2)) << 1;
        vo[i] = k * 8192 + (((lane & 15) ^ f) << 4) + 4096 - (i & 3) * 1024; }
      sb = wq + head * 256 - 4096; tstride = 64 * 8192;
    }
    auto issueT = [&](int t, unsigned slot) { const char* s = sb + ((t + kt0) & 15) * tstride; const unsigned l = L_RING + slot + lw * 8192;
      dma4(vo[0], vo[1], vo[2], vo[3], s, l); dma4(vo[4], vo[5], vo[6], vo[7], s, l + 4096); };
    issueT(0, slot_off(0)); issueT(1, slot_off(1));
#pragma unroll
    for (int i = 0; i < 8; ++i) { const int row = i * 4 + (lane >> 4); vk[i] = row * 256 + (((lane & 15) ^ (row & 15)) << 4) + 4096 - (i & 3) * 1024; }
#pragma unroll
    for (int i = 0; i < 8; ++i) { const int f = ((lane >> 4) & 1) | ((i & 1) << 1);
      vw[i] = (lw * 64 + i * 8 + (lane >> 3)) * 2048 + (((lane & 7) ^ (f << 1)) << 4) + 4096 - (i & 3) * 1024; }
    const unsigned vv0 = (lw & 1) ? vk[4] : vk[0], vv1 = (lw & 1) ? vk[5] : vk[1], vv2 = (lw & 1) ? vk[6] : vk[2], vv3 = (lw & 1) ? vk[7] : vk[3];
    auto issueW = [&](int j) { const int hp = (((j >> 2) + g0) & 3) * 4 + ((j + 2 * par) & 3); const char* s = wo + (size_t)hp * 256 * 2048 + ct * 128 - 4096;
      const unsigned l = L_RING + slot_off(16 + j) + lw * 8192;
      dma4(vw[0], vw[1], vw[2], vw[3], s, l); dma4(vw[4], vw[5], vw[6], vw[7], s, l + 4096); };
    auto issueV = [&](int g) { const int gg = (g + g0) & 3; const char* s = vc + (size_t)((rp * 2 + (lw >> 1)) * 4 + gg) * 8192 - 4096;
      dma4(vv0, vv1, vv2, vv3, s, L_V + lw * 4096); };
    auto issueP = [&](int m) { const int h4 = (((m >> 1) + g0) & 3) * 8 + ((m + par) & 1) * 4; const char* s = wsP + (size_t)((rp * 2 + (lw >> 1)) * 32 + h4 + (lw & 1) * 2) * 2048;
      dma4_sc1(lane * 16, s, L_KP + (m & 1) * 16384 + lw * 4096); };
    GAS unsigned* const gN = (GAS unsigned*)(a.ws + WS_N) + (br * 4 + rb) * 32;
    GAS unsigned* const gI = (GAS unsigned*)(a.ws + WS_I) + (br * 4 + rb) * 1024;
    GAS unsigned* const myN = gN + (lane & 31);
    GAS unsigned* const myI = gI + head * 32 + (lane & 31);
    GAS unsigned* const outI = gI + (lane & 31) * 32 + head;
    unsigned entry, poll0, nfin, nonce, nused;
    auto aload = [&](unsigned& d, GAS unsigned* p) { asm volatile("global_load_dword %0, %1, off sc1" : "=v"(d) : "v"(p) : "memory"); };
    auto landed = [&](unsigned& d) { asm volatile("" : "+v"(d)); };
    auto astore = [&](GAS unsigned* p, unsigned v) { asm volatile("global_store_dword %0, %1, off sc1" :: "v"(p), "v"(v) : "memory"); };
    static_for<0, 14>([&](auto nn) {
      constexpr int n = nn.value;
      waitv<8>(); bar();
      if constexpr (n == 0) aload(entry, myI);
      if constexpr (n == 1) {
        landed(entry);
        if (lw == 0) {
          unsigned long long live = 0xffffffffull; unsigned nv = 0;
#pragma unroll
          for (int b = 0; b < 6; ++b) {
            const unsigned long long one = __builtin_amdgcn_ballot_w64((entry >> b) & 1u) & live, zero = live & ~one;
            const bool pick1 = __builtin_popcountll(one) < __builtin_popcountll(zero);
            nv |= pick1 ? (1u << b) : 0u; live = pick1 ? one : zero;
          }
          astore(gN + head, nv);
          *(volatile LAS unsigned*)(size_t)(L_M + 16) = nv;
          asm volatile("s_waitcnt lgkmcnt(0)" ::: "memory");
        }
      }
      if constexpr (n == 3) nonce = *(volatile LAS unsigned*)(size_t)(L_M + 16);
      issueT(n + 2, slot_off(n + 2));
    });
    waitv<8>(); bar();
    { const char* s = kc + (size_t)((rb * 4 + lw) * 4 + hg) * 8192 - 4096;
      dma4(vk[0], vk[1], vk[2], vk[3], s, L_KP + lw * 8192); dma4(vk[4], vk[5], vk[6], vk[7], s, L_KP + lw * 8192 + 4096); }
    waitv<8>(); bar();
    aload(nused, myN);
    issueW(0);
    waitv<8>(); bar(); issueV(0); issueW(1);
    waitv<8>(); bar(); issueW(2);
    static_for<1, 16>([&](auto jj) {
      constexpr int j = jj.value;
      if constexpr (j + 1 <= 15) waitv<8>(); else waitv<0>();
      bar();
      if constexpr (j == 1) { landed(nused); if (lw == 0) astore(outI, nused); }
      if constexpr (j == LT - 2) aload(poll0, myI);
      if constexpr (j == LT) aload(nfin, myN);
      if constexpr (j == LT + 1) { landed(nfin); if (lw == 0 && __builtin_amdgcn_ballot_w64(nfin != nused) != 0) astore(outI, nfin); }
      if constexpr (j % 4 == 1 && j <= 9) issueV(j / 4 + 1);
      if constexpr (j == LT - 1) {
        landed(poll0);
        if (__builtin_amdgcn_ballot_w64(poll0 != nonce) == 0) {
          issueP(0); issueW(j + 2);
        } else {
          issueW(j + 2);
          _Pragma("unroll 1") for (unsigned spins = 0; spins < (1u << 20); ++spins) {
            unsigned f, nn2;
            asm volatile("global_load_dword %0, %2, off sc1\n\tglobal_load_dword %1, %3, off sc1\n\ts_waitcnt vmcnt(0)"
                         : "=&v"(f), "=&v"(nn2) : "v"(myI), "v"(myN) : "memory");
            if (__builtin_amdgcn_ballot_w64(f != nonce) == 0) break;
            if (lw == 0 && __builtin_amdgcn_ballot_w64(nn2 != nused) != 0) { nused = nn2; astore(outI, nused); }
            __builtin_amdgcn_s_sleep(2);
          }
          issueP(0); waitv<0>();
        }
      } else {
        constexpr int m = j + 1 - LT;
        if constexpr (m >= 0 && m <= 7) issueP(m);
        if constexpr (j + 2 <= 15) issueW(j + 2);
      }
    });
  } else {
    STAMP(0);
    __builtin_amdgcn_s_setprio(1);
    const int g = lane >> 4, q4 = (lane & 15) >> 2, p = lane & 3, r = lane & 15;
    const int rh = w >> 1, dh = w & 1;
    s8 Pfrag[2];
    {
      unsigned aA[2][4], aB[2][2][2];
#pragma unroll
      for (int c = 0; c < 4; ++c) { aA[0][c] = opaque(L_RING + 16384 + (8 * g + q4) * 256 + (((dh * 4 + c) ^ (q4 | ((g & 1) << 2))) << 5) + p * 8); aA[1][c] = opaque(aA[0][c] + 65536); }
#pragma unroll
      for (int kk = 0; kk < 2; ++kk)
#pragma unroll
        for (int o = 0; o < 2; ++o) { const int row = rh * 64 + ((o ^ dh ^ 1) << 5) + r;
          aB[0][kk][o] = opaque(L_RING + row * 128 + (((4 * kk + g) ^ ((row >> 1) & 7)) << 4)); aB[1][kk][o] = opaque(aB[0][kk][o] + 65536); }
      f4v acc[4][4];
      s4 Al[2][4], Ah[2][4]; s8 B[2][4];
      auto step = [&](auto first, auto bufc, auto rdn, auto tile, auto kkc) {
        constexpr int buf = bufc.value, nb = buf ^ 1, kk = kkc.value;
        constexpr unsigned so = slot_off(tile.value); constexpr int hi = so >= 65536; constexpr unsigned o = so - hi * 65536;
        static_for<0, 16>([&](auto mm) {
          constexpr int m = mm.value, c = m >> 2, b = m & 3;
          SB();
          if constexpr (first.value) mma0(acc[c][b], cat(Al[buf][c], Ah[buf][c]), B[buf][b]);
          else if constexpr (m == 0) mma(acc[c][b], cat(Al[buf][c], Ah[buf][c]), B[buf][b], B[buf][1], B[buf][2], B[buf][3], cat(Al[buf][1], Ah[buf][1]));
          else if constexpr (m == 8) mma(acc[c][b], cat(Al[buf][c], Ah[buf][c]), B[buf][b], cat(Al[buf][3], Ah[buf][3]));
          else mma(acc[c][b], cat(Al[buf][c], Ah[buf][c]), B[buf][b]);
          SB();
          if constexpr (rdn.value) {
            if constexpr (m == 0) B[nb][0] = rd16(aB[hi][kk][0] + o);
            if constexpr (m == 1) Al[nb][0] = tr(aA[hi][0] + o + kk * 8192);
            if constexpr (m == 2) Ah[nb][0] = tr(aA[hi][0] + o + kk * 8192 + 1024);
            if constexpr (m >= 3 && m <= 5) B[nb][m - 2] = rd16(aB[hi][kk][(m - 2) >> 1] + o + ((m - 2) & 1) * 2048);
            if constexpr (m >= 6 && m < 12) { constexpr int cc = 1 + (m - 6) / 2;
              if constexpr (m & 1) Ah[nb][cc] = tr(aA[hi][cc] + o + kk * 8192 + 1024); else Al[nb][cc] = tr(aA[hi][cc] + o + kk * 8192); }
          }
        });
        SB();
      };
      *(volatile LAS unsigned*)(size_t)(L_M + w * 4) = 0u;
      barl();
      STAMP(1);
#pragma unroll
      for (int c = 0; c < 4; ++c) { Al[0][c] = tr(aA[0][c]); Ah[0][c] = tr(aA[0][c] + 1024); B[0][c] = rd16(aB[0][0][c >> 1] + (c & 1) * 2048); }
      static_for<0, 16>([&](auto t) {
        step(IC<(t.value == 0)>{}, IC<0>{}, IC<1>{}, t, IC<1>{});
        barl();
        if constexpr (t.value == 7) STAMP(2);
        if constexpr (t.value == 15) STAMP(3);
        step(IC<0>{}, IC<1>{}, IC<(t.value < 15)>{}, IC<t.value + 1>{}, IC<0>{});
      });
      s8 Kf[2][2][2];
#pragma unroll
      for (int bl = 0; bl < 2; ++bl)
#pragma unroll
        for (int kb = 0; kb < 2; ++kb)
#pragma unroll
          for (int s = 0; s < 2; ++s) {
            const unsigned rowb = L_KP + ((rh * 2 + (bl ^ dh ^ 1)) * 32 + kb * 16 + r) * 256 + (g & 1) * 8;
            const int ch = dh * 8 + s * 4 + (g >> 1);
            Kf[bl][kb][s] = cat(rd8(rowb + ((ch ^ r) << 4)), rd8(rowb + (((ch + 2) ^ r) << 4)));
          }
      SB();
      mfma_done(acc[3][0], acc[3][1], acc[3][2], acc[3][3]);
      pin4(acc[0][0], acc[0][1], acc[0][2], acc[0][3]); pin4(acc[1][0], acc[1][1], acc[1][2], acc[1][3]); pin4(acc[2][0], acc[2][1], acc[2][2], acc[2][3]);
      f4v St[2][2][2];
#pragma unroll
      for (int o = 0; o < 2; ++o) {
#pragma unroll
        for (int qb = 0; qb < 2; ++qb) {
          const s8 q0 = pack8(acc[0][2 * o + qb], acc[1][2 * o + qb]), q1 = pack8(acc[2][2 * o + qb], acc[3][2 * o + qb]);
          const f4v z = {0.f, 0.f, 0.f, 0.f};
          St[o][0][qb] = mfma(Kf[o][0][1], q1, mfma(Kf[o][0][0], q0, z));
          St[o][1][qb] = mfma(Kf[o][1][1], q1, mfma(Kf[o][1][0], q0, z));
        }
        if (o == 0) {
#pragma unroll
          for (int t = 0; t < 4; ++t) *(LAS f4v*)(size_t)(L_X + w * 4096 + t * 1024 + lane * 16) = St[0][t >> 1][t & 1];
          asm volatile("s_waitcnt lgkmcnt(0)" ::: "memory");
          *(volatile LAS unsigned*)(size_t)(L_M + w * 4) = 1u;
        }
      }
      while (__builtin_amdgcn_readfirstlane(*(volatile LAS unsigned*)(size_t)(L_M + (w ^ 1) * 4)) == 0u) {}
      asm volatile("" ::: "memory");
      STAMP(4);
#pragma unroll
      for (int t = 0; t < 4; ++t) St[1][t >> 1][t & 1] += *(LAS f4v*)(size_t)(L_X + (w ^ 1) * 4096 + t * 1024 + lane * 16);
      const float cs = 0.08838834764831845f * 1.4426950408889634f;
#pragma unroll
      for (int qb = 0; qb < 2; ++qb) {
        f4v u = St[1][0][qb], v = St[1][1][qb];
        const float mx = xmax(fmaxf(fmaxf(fmaxf(u[0], u[1]), fmaxf(u[2], u[3])), fmaxf(fmaxf(v[0], v[1]), fmaxf(v[2], v[3]))));
        const float mc = mx * cs;
#pragma unroll
        for (int i = 0; i < 4; ++i) { u[i] = __builtin_amdgcn_exp2f(__builtin_fmaf(u[i], cs, -mc)); v[i] = __builtin_amdgcn_exp2f(__builtin_fmaf(v[i], cs, -mc)); }
        const float rs = __builtin_amdgcn_rcpf(xsum(((u[0] + u[1]) + (u[2] + u[3])) + ((v[0] + v[1]) + (v[2] + v[3]))));
        Pfrag[qb] = pack8(u * rs, v * rs);
      }
    }
    const int pb = rb * 4 + w;
    {
      const __amdgpu_buffer_rsrc_t rs = __builtin_amdgcn_make_buffer_rsrc(wsP + (size_t)(pb * 32 + head) * 2048, 0, 2048, 0x00020000);
      __builtin_amdgcn_raw_buffer_store_b128(__builtin_bit_cast(v4u, Pfrag[0]), rs, lane * 16, 0, 16);
      __builtin_amdgcn_raw_buffer_store_b128(__builtin_bit_cast(v4u, Pfrag[1]), rs, lane * 16 + 1024, 0, 16);
    }
    const int b2 = w >> 1, c2 = w & 1;
    unsigned aW[2][2];
#pragma unroll
    for (int nb = 0; nb < 2; ++nb) { aW[0][nb] = opaque(L_RING + (8 * g + q4) * 128 + (((c2 * 2 + nb) ^ ((q4 >> 1) | ((g & 1) << 1))) << 5) + p * 8); aW[1][nb] = opaque(aW[0][nb] + 65536); }
    const unsigned aP = opaque(L_KP + b2 * 8192 + lane * 16);
    const unsigned aV = opaque(L_V + (b2 * 32 + r) * 256 + ((g ^ r) << 4));
    s8 Vf[2][4];
    s4 Wl[3][2], Wh[3][2];
    f4v X[2][2][2];
    unsigned Xb[32][2][4];
    s8 Pf[2][2];
    f4v out[2][2];
    auto rdW = [&](auto uu, auto piece) {
      constexpr int u = uu.value, tile = 16 + (u >> 3), pc = piece.value;
      constexpr unsigned so = slot_off(tile); constexpr int hi = so >= 65536;
      constexpr unsigned o = so - hi * 65536 + (u & 7) * 4096 + (pc & 1) * 512;
      if constexpr (pc & 1) Wh[u % 3][pc >> 1] = tr(aW[hi][pc >> 1] + o); else Wl[u % 3][pc >> 1] = tr(aW[hi][pc >> 1] + o);
    };
    auto rdV = [&](auto kb, auto s) { Vf[kb.value][s.value] = rd16((aV ^ (s.value << 6)) + kb.value * 4096); };
    auto rdP = [&](auto hh, auto qb) { constexpr int h = hh.value; Pf[h & 1][qb.value] = rd16(aP + ((h >> 2) & 1) * 16384 + (h & 3) * 2048 + qb.value * 1024); };
    auto cvt = [&](auto hh, auto ii) { constexpr int h = hh.value, i = ii.value, nb = i >> 2, kb = (i >> 1) & 1, e = (i & 1) * 2;
      Xb[h][nb][i & 3] = opaque(pk(X[h & 1][kb][nb][e], X[h & 1][kb][nb][e + 1])); };
    auto pvm = [&](auto hh, auto ii) { constexpr int h = hh.value, nb = ii.value >> 1, qb = ii.value & 1;
      v4u xa = {Xb[h][nb][0], Xb[h][nb][1], Xb[h][nb][2], Xb[h][nb][3]};
      if constexpr (h == 0) { if constexpr (ii.value == 0) mma0(out[nb][qb], __builtin_bit_cast(s8, xa), Pf[h & 1][qb], Pf[h & 1][1]); else mma0(out[nb][qb], __builtin_bit_cast(s8, xa), Pf[h & 1][qb]); }
      else { if constexpr (ii.value == 0) mma(out[nb][qb], __builtin_bit_cast(s8, xa), Pf[h & 1][qb], Pf[h & 1][1]); else mma(out[nb][qb], __builtin_bit_cast(s8, xa), Pf[h & 1][qb]); } };
    STAMP(5);
    barl();
    STAMP(6);
    static_for<0, 4>([&](auto s) { rdV(IC<0>{}, s); rdV(IC<1>{}, s); if constexpr (s.value < 2) static_for<0, 4>([&](auto pc) { rdW(s, pc); }); });
    static_for<0, 128>([&](auto uu) {
      constexpr int u = uu.value, h = u >> 2, s = u & 3, j = u >> 3;
      constexpr bool cross = ((u + 2) & 7) == 0 && u + 2 < 128;
      static_for<0, 4>([&](auto mm) {
        constexpr int m = mm.value, nb = m >> 1, kb = m & 1;
        SB();
        if constexpr (s == 0) {
          if constexpr (m == 0) mma0(X[h & 1][kb][nb], Vf[kb][s], cat(Wl[u % 3][nb], Wh[u % 3][nb]), cat(Wl[u % 3][1], Wh[u % 3][1]));
          else mma0(X[h & 1][kb][nb], Vf[kb][s], cat(Wl[u % 3][nb], Wh[u % 3][nb]));
        } else {
          if constexpr (m == 0) mma(X[h & 1][kb][nb], Vf[kb][s], cat(Wl[u % 3][nb], Wh[u % 3][nb]), cat(Wl[u % 3][1], Wh[u % 3][1]));
          else mma(X[h & 1][kb][nb], Vf[kb][s], cat(Wl[u % 3][nb], Wh[u % 3][nb]));
        }
        SB();
        if constexpr (cross) {
          if constexpr (m == 1) {
            if constexpr (j == 0) waitv<0>();
            barl();
            if constexpr (j == 0) STAMP(7);
            if constexpr (j == LT - 2) STAMP(8);
            if constexpr (j == LT - 1) STAMP(9);
            if constexpr (j == 14) STAMP(10);
            rdW(IC<u + 2>{}, IC<0>{});
          }
          if constexpr (m == 2) { rdW(IC<u + 2>{}, IC<1>{}); rdW(IC<u + 2>{}, IC<2>{}); }
          if constexpr (m == 3) rdW(IC<u + 2>{}, IC<3>{});
        } else if constexpr (u + 2 < 128) rdW(IC<u + 2>{}, mm);
        if constexpr (h >= 1) { constexpr int i = s * 4 + m - 2; if constexpr (i >= 0 && i < 8) cvt(IC<h - 1>{}, IC<i>{}); }
        if constexpr ((h & 7) == 7 && h < 31 && m >= 2) rdV(IC<kb>{}, IC<s>{});
      });
      if constexpr (exec_at(u) >= 0) static_for<0, 4>([&](auto ii) { SB(); pvm(IC<exec_at(u)>{}, ii); SB(); });
      if constexpr (read_at(u) >= 0) { rdP(IC<read_at(u)>{}, IC<0>{}); rdP(IC<read_at(u)>{}, IC<1>{}); }
    });
    SB();
    mfma_done(X[1][0][0], X[1][0][1], X[1][1][0], X[1][1][1]);
    static_for<0, 8>([&](auto ii) { cvt(IC<31>{}, ii); });
    SB(); asm volatile("s_nop 1"); SB();
    static_for<0, 4>([&](auto ii) { pvm(IC<31>{}, ii); });
    SB();
    mfma_done(out[0][0], out[0][1], out[1][0], out[1][1]);
    const size_t orow = (size_t)((rp * 2 + b2) * 32 + r) * 4096 + br * 2048 + ct * 128 + c2 * 64 + ((g & 1) * 16 + (g >> 1) * 8) * 2;
#pragma unroll
    for (int qb = 0; qb < 2; ++qb) {
      v2u s0 = __builtin_amdgcn_permlane16_swap(pk(out[0][qb][0], out[0][qb][1]), pk(out[1][qb][0], out[1][qb][1]), false, false);
      v2u s1 = __builtin_amdgcn_permlane16_swap(pk(out[0][qb][2], out[0][qb][3]), pk(out[1][qb][2], out[1][qb][3]), false, false);
      v4u o4 = {s0[0], s1[0], s0[1], s1[1]};
      *(GAS v4u*)(a.out + orow + (size_t)qb * 16 * 4096) = o4;
    }
  }
#endif
}

extern "C" void kernel_launch(void* const* d_in, const int* in_sizes, int n_in, void* d_out, int out_size, void* d_ws, size_t ws_size,
                              hipStream_t stream) {
  Args a;
  for (int b = 0; b < 2; ++b) {
    a.x[b] = (const char*)d_in[b * 5 + 0]; a.k[b] = (const char*)d_in[b * 5 + 1]; a.v[b] = (const char*)d_in[b * 5 + 2];
    a.wq[b] = (const char*)d_in[b * 5 + 3]; a.wo[b] = (const char*)d_in[b * 5 + 4];
  }
  a.out = (char*)d_out; a.ws = (char*)d_ws;
  (void)hipFuncSetAttribute((const void*)gqa_fused_kernel, hipFuncAttributeMaxDynamicSharedMemorySize, LDS_BYTES);
  gqa_fused_kernel<<<256, 512, LDS_BYTES, stream>>>(a);
}
```

```cpp
#include <hip/hip_runtime.h>
#include <hip/hip_bf16.h>
#include <stdint.h>

typedef unsigned v4u __attribute__((ext_vector_type(4)));
typedef unsigned v2u __attribute__((ext_vector_type(2)));
typedef short s4 __attribute__((ext_vector_type(4)));
typedef short s8 __attribute__((ext_vector_type(8)));
typedef __bf16 bf8 __attribute__((ext_vector_type(8)));
typedef __bf16 bf2 __attribute__((ext_vector_type(2)));
typedef float f4v __attribute__((ext_vector_type(4)));
typedef float f2v __attribute__((ext_vector_type(2)));
#define LAS __attribute__((address_space(3)))
#define GAS __attribute__((address_space(1)))
#define DI __device__ __forceinline__
#define SB() __builtin_amdgcn_sched_barrier(0)

#define CPRIO 1
#define LPRIO 0
#define OAUX 0
#define PMODE 0
#define EARLYV 1
#define LT 7
#define TOUCH0 15
#define STAG1 13
#define STAG2 4

constexpr unsigned L_KP = 0;
constexpr unsigned L_RING = 32768;
constexpr unsigned L_V = 131072;
constexpr unsigned L_M = 147456;
constexpr unsigned L_X = L_RING;
constexpr unsigned LDS_BYTES = 163840;
constexpr size_t WS_N = 0, WS_I = 4096, WS_P = 65536;

template <int N> struct IC { static constexpr int value = N; };
template <int I, int N, class F> DI void static_for(F&& f) { if constexpr (I < N) { f(IC<I>{}); static_for<I + 1, N>(f); } }

#define GL4(P) "global_load_lds_dwordx4 %0, %4" P "\n\tglobal_load_lds_dwordx4 %1, %4 offset:1024" P "\n\t" \
               "global_load_lds_dwordx4 %2, %4 offset:2048" P "\n\tglobal_load_lds_dwordx4 %3, %4 offset:3072" P
struct Piece { unsigned v0, v1, v2, v3; const char* sb; unsigned lds; };
template <bool SC1> DI void dma4(const Piece& p) {
  if constexpr (SC1) asm volatile("s_mov_b32 m0, %5\n\ts_nop 0\n\t" GL4(" sc1") :: "v"(p.v0), "v"(p.v1), "v"(p.v2), "v"(p.v3), "s"(p.sb), "s"(p.lds) : "memory");
  else asm volatile("s_mov_b32 m0, %5\n\ts_nop 0\n\t" GL4("") :: "v"(p.v0), "v"(p.v1), "v"(p.v2), "v"(p.v3), "s"(p.sb), "s"(p.lds) : "memory");
}
template <int N, bool SC1> DI void wb_dma4(const Piece& p) {
  if constexpr (SC1) asm volatile("s_mov_b32 m0, %5\n\ts_waitcnt vmcnt(%6)\n\ts_barrier\n\t" GL4(" sc1") :: "v"(p.v0), "v"(p.v1), "v"(p.v2), "v"(p.v3), "s"(p.sb), "s"(p.lds), "i"(N) : "memory");
  else asm volatile("s_mov_b32 m0, %5\n\ts_waitcnt vmcnt(%6)\n\ts_barrier\n\t" GL4("") :: "v"(p.v0), "v"(p.v1), "v"(p.v2), "v"(p.v3), "s"(p.sb), "s"(p.lds), "i"(N) : "memory");
}
DI void touch(const char* p, unsigned lds) {
  asm volatile("s_mov_b32 m0, %1\n\ts_nop 0\n\tglobal_load_lds_dword %0, off" :: "v"(p), "s"(lds) : "memory");
}
DI void acquire() { if (PMODE == 1) asm volatile("buffer_inv sc1" ::: "memory"); }
template <int N> DI void waitv() {
 asm volatile("s_waitcnt vmcnt(%0)" ::"i"(N) : "memory"); }
DI void bar() { asm volatile("s_barrier" ::: "memory"); }
DI void barl() { asm volatile("s_waitcnt lgkmcnt(0)\n\ts_barrier" ::: "memory"); }
DI s4 tr(unsigned a) { return __builtin_amdgcn_ds_read_tr16_b64_v4i16((LAS s4*)(size_t)a); }
DI s8 cat(s4 lo, s4 hi) { return __builtin_shufflevector(lo, hi, 0, 1, 2, 3, 4, 5, 6, 7); }
DI s8 rd16(unsigned a) { return *(LAS s8*)(size_t)a; }
DI s4 rd8(unsigned a) { return *(LAS s4*)(size_t)a; }
DI f4v mfma(s8 a, s8 b, f4v c) { return __builtin_amdgcn_mfma_f32_16x16x32_bf16(__builtin_bit_cast(bf8, a), __builtin_bit_cast(bf8, b), c, 0, 0, 0); }
#define MF "v_mfma_f32_16x16x32_bf16 %0, %1, %2, "
DI void mma(f4v& c, s8 a, s8 b) { asm volatile(MF "%0" : "+v"(c) : "v"(a), "v"(b)); }
DI void mma(f4v& c, s8 a, s8 b, s8 e0) { asm volatile(MF "%0" : "+v"(c) : "v"(a), "v"(b), "v"(e0)); }
DI void mma(f4v& c, s8 a, s8 b, s8 e0, s8 e1, s8 e2, s8 e3) { asm volatile(MF "%0" : "+v"(c) : "v"(a), "v"(b), "v"(e0), "v"(e1), "v"(e2), "v"(e3)); }
DI void mma(f4v& c, s8 a, s8 b, s8 e0, s8 e1) { asm volatile(MF "%0" : "+v"(c) : "v"(a), "v"(b), "v"(e0), "v"(e1)); }
DI void mma(f4v& c, s8 a, s8 b, s8 e0, s8 e1, s8 e2) { asm volatile(MF "%0" : "+v"(c) : "v"(a), "v"(b), "v"(e0), "v"(e1), "v"(e2)); }
DI void mma0(f4v& c, s8 a, s8 b) { asm volatile(MF "0" : "=&v"(c) : "v"(a), "v"(b)); }
DI void mma0(f4v& c, s8 a, s8 b, s8 e0) { asm volatile(MF "0" : "=&v"(c) : "v"(a), "v"(b), "v"(e0)); }
DI void mfma_done(f4v& a, f4v& b, f4v& c, f4v& d) { asm volatile("s_nop 7\n\ts_nop 1" : "+v"(a), "+v"(b), "+v"(c), "+v"(d)); }
DI void pin4(f4v& a, f4v& b, f4v& c, f4v& d) { asm volatile("" : "+v"(a), "+v"(b), "+v"(c), "+v"(d)); }
DI unsigned pk(float a, float b) { f2v v = {a, b}; return __builtin_bit_cast(unsigned, __builtin_convertvector(v, bf2)); }
DI s8 pack8(f4v a, f4v b) { v4u r = {pk(a[0], a[1]), pk(a[2], a[3]), pk(b[0], b[1]), pk(b[2], b[3])}; return __builtin_bit_cast(s8, r); }
DI unsigned opaque(unsigned x) { asm volatile("" : "+v"(x)); return x; }
DI unsigned vmax(unsigned a, unsigned b) { unsigned r; asm("v_max_f32 %0, %1, %2" : "=v"(r) : "v"(a), "v"(b)); return r; }
constexpr unsigned slot_off(int n) { return (unsigned)(n % 3) * 32768u; }
constexpr int pv_unit(int p) { const int e = 4 * (LT + (p >> 1)) + 2 * (p & 1); return e > 4 * p + 4 ? e : 4 * p + 4; }
constexpr int pair_at(int u) { for (int p = 0; p < 16; ++p) if (pv_unit(p) == u || (u == 63 && p == 15)) return p; return -1; }
constexpr int p_issue(int m) { const int a = LT + m - 1, b = m >= 2 ? (pv_unit(2 * m - 3) + 1) / 4 + 1 : 0; return a > b ? a : b; }
constexpr int p_at(int j) { for (int m = 0; m < 8; ++m) if (p_issue(m) == j) return m; return -1; }
constexpr bool pv_ok() {
  for (int m = 0; m < 8; ++m) if (4 * (p_issue(m) + 1) > pv_unit(2 * m) || (m > 0 && p_issue(m) <= p_issue(m - 1))) return false;
  return pv_unit(14) < 63 && pv_unit(15) == 64 && p_issue(7) <= 13 && LT >= 4; }
static_assert(pv_ok(), "P schedule");

#define STAMP(i)

struct Args {
  const char* x[2]; const char* k[2]; const char* v[2]; const char* wq[2]; const char* wo[2];
  char* out; char* ws;
};

__global__ __launch_bounds__(512, 2) void gqa_fused_kernel(Args a) {
#if __HIP_DEVICE_COMPILE__
  const int lane = threadIdx.x & 63;
  const int w = __builtin_amdgcn_readfirstlane(threadIdx.x >> 6);
  const int xcd = blockIdx.x & 7, idx = blockIdx.x >> 3;
  const int br = xcd >> 2, hg = xcd & 3, i8 = idx & 7, rb = idx >> 3;
  const int head = hg * 8 + i8;
  const int ct = hg * 4 + (i8 & 3), rp = rb * 2 + (i8 >> 2);
  const int ord = STAG2 == 1 ? ((rp + 2 * ct) & 7) : STAG2 == 2 ? ((rp + 2 * ct) & 6) : 0;
  const int g0 = STAG2 >= 4 ? (ct + 2 * rp) & 3 : ord >> 1;
  const int rot2 = STAG2 >= 4 ? (rp >> 1) & 3 : (ord & 1) * 2;
  const bool late = STAG2 == 4 ? (ct & 2) != 0 : STAG2 == 6;
  auto grp = [&](int k) { return (g0 + (late ? ((k >> 1) | ((k & 1) << 1)) : k)) & 3; };
  const int kt0 = STAG1 == 3 ? 8 * ((rb ^ i8) & 1) : STAG1 == 4 ? 8 * ((rb ^ i8) & 1) + 2 * hg : STAG1 == 10 || STAG1 == 13 ? 4 * hg : STAG1 == 12 ? 2 * hg : 0;
  const int rot1 = STAG1 == 13 ? rb + (i8 & 1) + (i8 >> 2) * 2 : STAG1 >= 9 ? rb + i8 : 0;
  const bool shift1 = STAG1 == 13 ? (i8 & 2) != 0 : (STAG1 == 9 || STAG1 == 10 || STAG1 == 12) && i8 >= 4;
  auto ktile = [&](int s) {
    const int plain = STAG1 >= 9 ? (s & 12) + ((s + rot1) & 3) : s;
    const int shifted = (s < 2 || s >= 14) ? (s & 14) + ((s + rot1) & 1) : 2 + ((s - 2) & 12) + ((s - 2 + rot1) & 3);
    return ((shift1 ? shifted : plain) + kt0) & 15; };
  char* const wsP = a.ws + WS_P + (size_t)br * (16 * 32 * 2048);

  if (w >= 4) {
    const int lw = w - 4;
    __builtin_amdgcn_s_setprio(LPRIO);
    const char* const x = br ? a.x[1] : a.x[0];
    const char* const wq = br ? a.wq[1] : a.wq[0];
    const char* const kc = br ? a.k[1] : a.k[0];
    const char* const vc = br ? a.v[1] : a.v[0];
    const char* const wo = br ? a.wo[1] : a.wo[0];
    unsigned vo[8], vk[8], vw[4];
    const char* sb; size_t tstride;
    if (lw < 2) {
#pragma unroll
      for (int i = 0; i < 8; ++i) { const int row = lw * 64 + i * 8 + (lane >> 3); vo[i] = row * 2048 + (((lane & 7) ^ ((row >> 1) & 7)) << 4) + 4096 - (i & 3) * 1024; }
      sb = x + (size_t)rb * 128 * 2048 - 4096; tstride = 128;
    } else {
#pragma unroll
      for (int i = 0; i < 8; ++i) { const int k = (lw - 2) * 32 + i * 4 + (lane >> 4); const int f = ((k & 3) | (((k >> 3) & 1) << 2)) << 1;
        vo[i] = k * 8192 + (((lane & 15) ^ f) << 4) + 4096 - (i & 3) * 1024; }
      sb = wq + head * 256 - 4096; tstride = 64 * 8192;
    }
    auto pT = [&](int t, int half) { return Piece{vo[4 * half], vo[4 * half + 1], vo[4 * half + 2], vo[4 * half + 3], sb + ktile(t) * tstride,
                                                  L_RING + slot_off(t) + lw * 8192 + half * 4096}; };
    dma4<false>(pT(0, 0)); dma4<false>(pT(0, 1));
    dma4<false>(pT(1, 0)); dma4<false>(pT(1, 1));
#pragma unroll
    for (int i = 0; i < 8; ++i) { const int row = i * 4 + (lane >> 4); vk[i] = row * 256 + (((lane & 15) ^ (row & 15)) << 4) + 4096 - (i & 3) * 1024; }
#pragma unroll
    for (int i = 0; i < 4; ++i) { const int f = ((lane >> 4) & 1) | ((i & 1) << 1);
      vw[i] = (lw * 32 + i * 8 + (lane >> 3)) * 2048 + (((lane & 7) ^ (f << 1)) << 4) + 4096 - i * 1024; }
    const unsigned vv0 = (lw & 1) ? vk[4] : vk[0], vv1 = (lw & 1) ? vk[5] : vk[1], vv2 = (lw & 1) ? vk[6] : vk[2], vv3 = (lw & 1) ? vk[7] : vk[3];
    const unsigned vp = lane * 16;
    auto pK = [&](int half) { return Piece{vk[4 * half], vk[4 * half + 1], vk[4 * half + 2], vk[4 * half + 3],
                                           kc + (size_t)((rb * 4 + lw) * 4 + hg) * 8192 - 4096, L_KP + lw * 8192 + half * 4096}; };
    auto pW = [&](int j, int half) { const int hp = grp(j >> 2) * 4 + ((j + rot2) & 3);
      return Piece{vw[0], vw[1], vw[2], vw[3], wo + (size_t)(hp * 2 + half) * 128 * 2048 + ct * 128 - 4096, L_RING + slot_off(16 + j) + half * 16384 + lw * 4096}; };
    auto pV = [&](int g) { const int gg = grp(g);
      return Piece{vv0, vv1, vv2, vv3, vc + (size_t)((rp * 2 + (lw >> 1)) * 4 + gg) * 8192 - 4096, L_V + lw * 4096}; };
    auto pP = [&](int m) { const int hp = grp(m >> 1) * 4 + ((2 * m + (lw & 1) + rot2) & 3);
      return Piece{vp, vp, vp, vp, wsP + (size_t)((rp * 2 + (lw >> 1)) * 32 + hp * 2) * 2048, L_KP + (m & 1) * 16384 + lw * 4096}; };
    GAS unsigned* const gN = (GAS unsigned*)(a.ws + WS_N) + (br * 4 + rb) * 32;
    GAS unsigned* const gI = (GAS unsigned*)(a.ws + WS_I) + (br * 4 + rb) * 1024;
    GAS unsigned* const myN = gN + (lane & 31);
    GAS unsigned* const myI = gI + head * 32 + (lane & 31);
    GAS unsigned* const outI = gI + (lane & 31) * 32 + head;
    unsigned entry, poll0, nfin, nonce, nused;
    auto aload = [&](unsigned& d, GAS unsigned* p) { asm volatile("global_load_dword %0, %1, off sc1" : "=v"(d) : "v"(p) : "memory"); };
    auto landed = [&](unsigned& d) { asm volatile("" : "+v"(d)); };
    auto astore = [&](GAS unsigned* p, unsigned v) { asm volatile("global_store_dword %0, %1, off sc1" :: "v"(p), "v"(v) : "memory"); };
    const int slowc = (3 - (int)((size_t)wo >> 7)) & 7;
    const bool towner = (slowc >> 2) == (hg & 1);
    const int tpiece = TOUCH0 >= 14 ? (rb * 2 + (i8 >> 2)) * 4 + lw : idx;
    const int tstep = TOUCH0 >= 14 ? (towner && (i8 & 3) == 0 ? (TOUCH0 == 14 ? 6 : lw < 2 ? 6 : 10) : -1) : (TOUCH0 > 0 && lw == 0 && towner) ? TOUCH0 + (idx >> 2) : -1;
    const int thp = STAG2 >= 4 ? ((slowc + 1 + 2 * (tpiece >> 4)) & 3) * 4 + ((tpiece >> 2) & 3) : (tpiece >> 3) * 4 + 2 + ((tpiece >> 2) & 1);
    const char* const tptr = wo + (size_t)((thp * 256 + (tpiece & 3) * 64 + lane) * 2048) + (hg * 4 + (slowc & 3)) * 128;
    const char* const kptr = kc + (size_t)((rb * 4 + lw) * 4 + hg) * 8192 + (i8 * 8 + (lane & 7)) * 128;
    static_for<0, 14>([&](auto nn) {
      constexpr int n = nn.value;
      if constexpr (EARLYV && n >= 9 && n <= 12) { if (shift1 == (n <= 10)) wb_dma4<13, false>(pT(n + 2, 0)); else wb_dma4<8, false>(pT(n + 2, 0)); }
      else wb_dma4<(n == 1 ? 9 : 8), false>(pT(n + 2, 0));
      dma4<false>(pT(n + 2, 1));
      if constexpr (EARLYV && (n == 8 || n == 10)) { if (shift1 == (n == 8)) { touch(kptr, L_M + 2048 + lw * 256); dma4<false>(pV(0)); } }
      if constexpr (n == 0) aload(entry, myI);
      if constexpr (n == 2) {
        landed(entry);
        if (lw == 0) {
          unsigned long long live = 0xffffffffull; unsigned nv = 0;
#pragma unroll
          for (int b = 0; b < 6; ++b) {
            const unsigned long long one = __builtin_amdgcn_ballot_w64((entry >> b) & 1u) & live, zero = live & ~one;
            const bool pick1 = __builtin_popcountll(one) < __builtin_popcountll(zero);
            nv |= pick1 ? (1u << b) : 0u; live = pick1 ? one : zero;
          }
          astore(gN + head, nv);
          *(volatile LAS unsigned*)(size_t)(L_M + 16) = nv;
          asm volatile("s_waitcnt lgkmcnt(0)" ::: "memory");
        }
      }
      if constexpr (n >= 2) { if (n == tstep) touch(tptr, L_M + 1024 + lw * 256); }
      if constexpr (n == 4) nonce = opaque(*(volatile LAS unsigned*)(size_t)(L_M + 16));
    });
    wb_dma4<8, false>(pK(0)); dma4<false>(pK(1));
    wb_dma4<8, false>(pW(0, 0)); dma4<false>(pW(0, 1));
    aload(nused, myN);
    if constexpr (EARLYV) wb_dma4<9, false>(pW(1, 0)); else { wb_dma4<9, false>(pV(0)); dma4<false>(pW(1, 0)); }
    dma4<false>(pW(1, 1));
    wb_dma4<8, false>(pW(2, 0)); dma4<false>(pW(2, 1));
    static_for<1, 16>([&](auto jj) {
      constexpr int j = jj.value;
      constexpr int m = p_at(j);
      constexpr bool hasP = m >= 0, hasV = j % 4 == 1 && j <= 9, hasW = j + 2 <= 15;
      constexpr int N = j == 15 ? 4 : j == LT + 1 ? 9 : 8;
      auto rest = [&](auto skip) {
        if constexpr (hasP && skip.value != 1) dma4<PMODE == 0>(pP(m));
        if constexpr (hasV && skip.value != 2) dma4<false>(pV(j / 4 + 1));
        if constexpr (hasW) { if constexpr (skip.value != 3) dma4<false>(pW(j + 2, 0)); dma4<false>(pW(j + 2, 1)); }
      };
      if constexpr (j == 1) {
        waitv<N>(); bar(); landed(nused);
        if (lw == 0) astore(outI, nused);
        STAMP(37);
        rest(IC<0>{});
      } else if constexpr (j == LT - 2) {
        waitv<N>(); bar(); aload(poll0, myI); rest(IC<0>{});
      } else if constexpr (j == LT - 1) {
        waitv<N>(); bar(); landed(poll0);
        if (__builtin_amdgcn_ballot_w64(poll0 != nonce) == 0) { STAMP(38); acquire(); rest(IC<0>{}); }
        else {
          rest(IC<1>{});
          _Pragma("unroll 1") for (unsigned spins = 0; spins < (1u << 20); ++spins) {
            unsigned f, nn2;
            asm volatile("global_load_dword %0, %2, off sc1\n\tglobal_load_dword %1, %3, off sc1\n\ts_waitcnt vmcnt(0)"
                         : "=&v"(f), "=&v"(nn2) : "v"(myI), "v"(myN) : "memory");
            if (__builtin_amdgcn_ballot_w64(f != nonce) == 0) break;
            if (lw == 0 && __builtin_amdgcn_ballot_w64(nn2 != nused) != 0) { nused = nn2; astore(outI, nused); }
            __builtin_amdgcn_s_sleep(2);
          }
          STAMP(38);
          acquire(); dma4<PMODE == 0>(pP(0)); waitv<0>();
        }
      } else if constexpr (hasP) { wb_dma4<N, PMODE == 0>(pP(m)); rest(IC<1>{}); }
      else if constexpr (hasV) { wb_dma4<N, false>(pV(j / 4 + 1)); rest(IC<2>{}); }
      else if constexpr (hasW) { wb_dma4<N, false>(pW(j + 2, 0)); rest(IC<3>{}); }
      else { waitv<N>(); bar(); }
      if constexpr (j == LT) aload(nfin, myN);
      if constexpr (j == LT + 2) { landed(nfin); if (lw == 0 && __builtin_amdgcn_ballot_w64(nfin != nused) != 0) astore(outI, nfin); }
    });
    waitv<0>(); bar();
  } else {
    STAMP(0);
    __builtin_amdgcn_s_setprio(CPRIO);
    const int g = lane >> 4, q4 = (lane & 15) >> 2, p = lane & 3, r = lane & 15;
    const int rh = w >> 1, dh = w & 1;
    s8 Pfrag[2];
    char* pbase = wsP + (size_t)((rb * 4 + w) * 32 + head) * 2048;
    char* obase = a.out + (size_t)((rp * 2 + (w >> 1)) * 32) * 4096 + br * 2048 + ct * 128 + (w & 1) * 64;
    asm volatile("" : "+s"(pbase), "+s"(obase));
    const unsigned ooff = opaque(r * 4096 + ((g & 1) * 16 + (g >> 1) * 8) * 2);
    const int b2 = w >> 1, c2 = w & 1;
    unsigned aW[2][2];
#pragma unroll
    for (int nb = 0; nb < 2; ++nb) { aW[0][nb] = opaque(L_RING + (8 * g + q4) * 128 + (((c2 * 2 + nb) ^ ((q4 >> 1) | ((g & 1) << 1))) << 5) + p * 8); aW[1][nb] = opaque(aW[0][nb] + 65536); }
    const unsigned aP = opaque(L_KP + b2 * 8192 + lane * 16);
    unsigned aV[4];
#pragma unroll
    for (int s = 0; s < 4; ++s) aV[s] = opaque(L_V + (b2 * 32 + r) * 256 + (((4 * s + g) ^ r) << 4));
    {
      unsigned aA[2][4], aB[2][2][2];
#pragma unroll
      for (int c = 0; c < 4; ++c) { aA[0][c] = opaque(L_RING + 16384 + (8 * g + q4) * 256 + (((dh * 4 + c) ^ (q4 | ((g & 1) << 2))) << 5) + p * 8); aA[1][c] = opaque(aA[0][c] + 65536); }
#pragma unroll
      for (int kk = 0; kk < 2; ++kk)
#pragma unroll
        for (int o = 0; o < 2; ++o) { const int row = rh * 64 + ((o ^ dh ^ 1) << 5) + r;
          aB[0][kk][o] = opaque(L_RING + row * 128 + (((4 * kk + g) ^ ((row >> 1) & 7)) << 4)); aB[1][kk][o] = opaque(aB[0][kk][o] + 65536); }
      f4v acc[4][4];
      s4 Al[4][2], Ah[4][2]; s8 B[2][4][2];
      auto rdA = [&](auto tile, auto cc, auto ii) { constexpr int c = cc.value, kk = ii.value >> 1;
        constexpr unsigned so = slot_off(tile.value); constexpr int hi = so >= 65536; constexpr unsigned o = so - hi * 65536 + kk * 8192;
        if constexpr (ii.value & 1) Ah[c][kk] = tr(aA[hi][c] + o + 1024); else Al[c][kk] = tr(aA[hi][c] + o); };
      auto rdB = [&](auto tile, auto bb, auto kk) { constexpr int b = bb.value;
        constexpr unsigned so = slot_off(tile.value); constexpr int hi = so >= 65536; constexpr unsigned o = so - hi * 65536;
        B[tile.value & 1][b][kk.value] = rd16(aB[hi][kk.value][b >> 1] + o + (b & 1) * 2048); };
      auto rdNext = [&](auto tile, auto ii) { constexpr int i = ii.value;
        if constexpr (i < 2) rdB(tile, IC<0>{}, ii); else if constexpr (i < 6) rdA(tile, IC<0>{}, IC<i - 2>{});
        else rdB(tile, IC<(i - 4) / 2>{}, IC<i & 1>{}); };
      auto mm1 = [&](auto tile, auto cc, auto bb, auto kc) { constexpr int t = tile.value, c = cc.value, b = bb.value, kk = kc.value;
        const s8 av = cat(Al[c][kk], Ah[c][kk]), bv = B[t & 1][b][kk];
        if constexpr (t == 0 && kk == 0) mma0(acc[c][b], av, bv);
        else if constexpr (kk == 0 && c == 0 && b == 0) mma(acc[c][b], av, bv, cat(Al[0][1], Ah[0][1]), B[t & 1][1][1]);
        else if constexpr (kk == 0 && c == 0 && b == 2) mma(acc[c][b], av, bv, B[t & 1][3][1]);
        else if constexpr (kk == 0 && c == 1 && b == 0) mma(acc[c][b], av, bv, cat(Al[1][1], Ah[1][1]));
        else if constexpr (kk == 0 && c == 2 && b == 0) mma(acc[c][b], av, bv, cat(Al[2][1], Ah[2][1]), cat(Al[3][0], Ah[3][0]), cat(Al[3][1], Ah[3][1]));
        else mma(acc[c][b], av, bv); };
      unsigned aK[2];
#pragma unroll
      for (int o = 0; o < 2; ++o) aK[o] = opaque(L_KP + ((rh * 2 + (o ^ dh ^ 1)) * 32 + r) * 256 + (g & 1) * 8 + (((dh * 8 + (g >> 1)) ^ r) << 4));
      unsigned aK2[2][3];
#pragma unroll
      for (int o = 0; o < 2; ++o)
#pragma unroll
        for (int i = 0; i < 3; ++i) aK2[o][i] = opaque(aK[o] ^ ((i + 1) << 5));
      const unsigned aXw = opaque(L_X + w * 4096 + lane * 16), aXr = opaque(L_X + (w ^ 1) * 4096 + lane * 16);
      const unsigned aFw = opaque(L_M + w * 4), aFr = opaque(L_M + (w ^ 1) * 4);
      *(volatile LAS unsigned*)(size_t)aFw = 0u;
      barl();
      STAMP(1);
      static_for<0, 12>([&](auto ii) { rdNext(IC<0>{}, ii); });
      static_for<0, 16>([&](auto t) {
        static_for<0, 16>([&](auto mm) { constexpr int m = mm.value;
          SB(); mm1(t, IC<(m >> 3)>{}, IC<((m >> 1) & 3)>{}, IC<(m & 1)>{}); SB();
          if constexpr (m < 12) rdA(t, IC<1 + (m >> 2)>{}, IC<(m & 3)>{}); });
        SB(); barl();
        STAMP(2 + t.value);
        if constexpr (t.value < 15) static_for<0, 16>([&](auto mm) { constexpr int m = mm.value;
          SB(); mm1(t, IC<2 + (m >> 3)>{}, IC<((m >> 1) & 3)>{}, IC<(m & 1)>{}); SB();
          if constexpr (m < 12) rdNext(IC<t.value + 1>{}, mm); });
      });
      s4 Kh[2][2][2][2];
      unsigned q[4][2][4];
      f4v St[2][2][2];
      auto rdK = [&](auto ii) { constexpr int i = ii.value, o = i >> 3, kb = (i >> 2) & 1, s = (i >> 1) & 1, u = i & 1, x = s * 2 + u;
        Kh[o][kb][s][u] = rd8((x == 0 ? aK[o] : aK2[o][x - 1]) + kb * 4096); };
      auto cvq = [&](auto bb, auto cc) { constexpr int b = bb.value, c = cc.value;
        q[b][c >> 1][(c & 1) * 2] = opaque(pk(acc[c][b][0], acc[c][b][1])); q[b][c >> 1][(c & 1) * 2 + 1] = opaque(pk(acc[c][b][2], acc[c][b][3])); };
      auto smm = [&](auto ii) { constexpr int i = ii.value, o = i >> 3, qb = (i >> 2) & 1, kb = (i >> 1) & 1, s = i & 1;
        const v4u qq = {q[2 * o + qb][s][0], q[2 * o + qb][s][1], q[2 * o + qb][s][2], q[2 * o + qb][s][3]};
        if constexpr (s == 0) mma0(St[o][kb][qb], cat(Kh[o][kb][s][0], Kh[o][kb][s][1]), __builtin_bit_cast(s8, qq));
        else mma(St[o][kb][qb], cat(Kh[o][kb][s][0], Kh[o][kb][s][1]), __builtin_bit_cast(s8, qq)); };
      static_for<0, 16>([&](auto mm) {
        constexpr int m = mm.value;
        constexpr int cb[15] = {0, 0, 1, 1, 0, 0, 2, 2, 3, 3, 1, 1, -1, 2, 2}, cc[15] = {0, 1, 0, 1, 2, 3, 0, 1, 0, 1, 2, 3, -1, 2, 3};
        SB(); mm1(IC<15>{}, IC<2 + ((m >> 1) & 1)>{}, IC<(m >> 2)>{}, IC<(m & 1)>{}); SB();
        rdK(mm);
        if constexpr (m < 15) { if constexpr (cb[m] >= 0) cvq(IC<cb[m]>{}, IC<cc[m]>{}); }
      });
      static_for<0, 16>([&](auto mm) {
        constexpr int m = mm.value;
        SB(); smm(mm); SB();
        if constexpr (m == 4) cvq(IC<3>{}, IC<2>{});
        if constexpr (m == 5) cvq(IC<3>{}, IC<3>{});
        if constexpr (m == 9 || m == 10 || m == 12 || m == 14) { constexpr int t = m == 9 ? 0 : m == 10 ? 1 : m == 12 ? 2 : 3;
          *(LAS f4v*)(size_t)(aXw + t * 1024) = St[0][t & 1][t >> 1]; }
      });
      SB();
      asm volatile("s_waitcnt lgkmcnt(0)" ::: "memory");
      *(volatile LAS unsigned*)(size_t)aFw = 1u;
      while (__builtin_amdgcn_readfirstlane(*(volatile LAS unsigned*)(size_t)aFr) == 0u) {}
      asm volatile("" ::: "memory");
      STAMP(18);
      pin4(St[1][0][0], St[1][0][1], St[1][1][0], St[1][1][1]);
#pragma unroll
      for (int t = 0; t < 4; ++t) St[1][t & 1][t >> 1] += *(LAS f4v*)(size_t)(aXr + t * 1024);
      const float cs = 0.08838834764831845f * 1.4426950408889634f;
      f4v e[2][2];
      float m2[2];
#pragma unroll
      for (int qb = 0; qb < 2; ++qb) { const f4v u = St[1][0][qb], v = St[1][1][qb];
        m2[qb] = fmaxf(fmaxf(fmaxf(fmaxf(u[0], u[1]), u[2]), fmaxf(fmaxf(u[3], v[0]), v[1])), fmaxf(v[2], v[3])); }
      v2u t0 = __builtin_amdgcn_permlane16_swap(__float_as_uint(m2[0]), __float_as_uint(m2[1]), false, false);
      unsigned z = vmax(t0[0], t0[1]);
      t0 = __builtin_amdgcn_permlane32_swap(z, z, false, false);
      z = __float_as_uint(__uint_as_float(vmax(t0[0], t0[1])) * -cs);
      t0 = __builtin_amdgcn_permlane16_swap(z, z, false, false);
      float s2[2];
#pragma unroll
      for (int qb = 0; qb < 2; ++qb) {
        const float mc = __uint_as_float(t0[qb]);
#pragma unroll
        for (int kb = 0; kb < 2; ++kb) {
          const f4v y = __builtin_elementwise_fma(St[1][kb][qb], (f4v){cs, cs, cs, cs}, (f4v){mc, mc, mc, mc});
#pragma unroll
          for (int i = 0; i < 4; ++i) e[qb][kb][i] = __builtin_amdgcn_exp2f(y[i]);
        }
        const f4v sm = e[qb][0] + e[qb][1];
        s2[qb] = (sm[0] + sm[2]) + (sm[1] + sm[3]);
      }
      t0 = __builtin_amdgcn_permlane16_swap(__float_as_uint(s2[0]), __float_as_uint(s2[1]), false, false);
      z = __float_as_uint(__uint_as_float(t0[0]) + __uint_as_float(t0[1]));
      t0 = __builtin_amdgcn_permlane32_swap(z, z, false, false);
      z = __float_as_uint(__builtin_amdgcn_rcpf(__uint_as_float(t0[0]) + __uint_as_float(t0[1])));
      t0 = __builtin_amdgcn_permlane16_swap(z, z, false, false);
#pragma unroll
      for (int qb = 0; qb < 2; ++qb) { const float rs = __uint_as_float(t0[qb]); Pfrag[qb] = pack8(e[qb][0] * rs, e[qb][1] * rs); }
    }
    {
      const __amdgpu_buffer_rsrc_t rs = __builtin_amdgcn_make_buffer_rsrc(pbase, 0, 2048, 0x00020000);
      __builtin_amdgcn_raw_buffer_store_b128(__builtin_bit_cast(v4u, Pfrag[0]), rs, lane * 16, 0, 16);
      __builtin_amdgcn_raw_buffer_store_b128(__builtin_bit_cast(v4u, Pfrag[1]), rs, lane * 16 + 1024, 0, 16);
    }
    s8 Vf[2][4];
    s4 Wl[4][4], Wh[4][4];
    f4v X[2][2];
    unsigned Xb[32][2][4];
    s8 Pf[2][2];
    f4v out[2][2];
    auto rdW = [&](auto rr) { constexpr int rho = rr.value, u = rho >> 3, s = (rho >> 1) & 3;
      constexpr unsigned so = slot_off(16 + (u >> 2)); constexpr int hi = so >= 65536;
      constexpr unsigned o = so - hi * 65536 + ((u >> 1) & 1) * 16384 + s * 4096 + (rho & 1) * 512;
      if constexpr (rho & 1) Wh[u & 3][s] = tr(aW[hi][u & 1] + o); else Wl[u & 3][s] = tr(aW[hi][u & 1] + o); };
    auto rdV = [&](auto kb, auto s) { Vf[kb.value][s.value] = rd16(aV[s.value] + kb.value * 4096); };
    auto rdP = [&](auto pp, auto ii) { constexpr int h = 2 * pp.value + (ii.value & 1), qb = ii.value >> 1;
      Pf[h & 1][qb] = rd16(aP + ((h >> 2) & 1) * 16384 + (h & 3) * 2048 + qb * 1024); };
    auto cvt = [&](auto uu, auto ii) { constexpr int u = uu.value, i = ii.value;
      Xb[u >> 1][u & 1][i] = opaque(pk(X[u & 1][i >> 1][(i & 1) * 2], X[u & 1][i >> 1][(i & 1) * 2 + 1])); };
    auto pvm = [&](auto pp, auto nn, auto qq, auto ee) { constexpr int h = 2 * pp.value + ee.value, nb = nn.value, qb = qq.value;
      const v4u xa = {Xb[h][nb][0], Xb[h][nb][1], Xb[h][nb][2], Xb[h][nb][3]};
      if constexpr (h == 0) mma0(out[nb][qb], __builtin_bit_cast(s8, xa), Pf[h & 1][qb]);
      else if constexpr (nb == 0 && ee.value == 0) mma(out[nb][qb], __builtin_bit_cast(s8, xa), Pf[h & 1][qb], Pf[1][qb]);
      else mma(out[nb][qb], __builtin_bit_cast(s8, xa), Pf[h & 1][qb]); };
    STAMP(19);
    barl();
    STAMP(20);
    static_for<0, 4>([&](auto s) { rdW(IC<2 * s.value>{}); rdW(IC<2 * s.value + 1>{}); rdV(IC<0>{}, s); });
    static_for<0, 4>([&](auto s) { rdV(IC<1>{}, s); });
    rdW(IC<8>{}); rdW(IC<9>{});
    static_for<0, 64>([&](auto uu) {
      constexpr int u = uu.value, pr = pair_at(u);
      static_for<0, 8>([&](auto ii) {
        constexpr int i = ii.value, kb = i >> 2, s = i & 3, rho = 8 * u + i + 10;
        const s8 wv = cat(Wl[u & 3][s], Wh[u & 3][s]);
        SB();
        if constexpr (i == 0) mma0(X[u & 1][kb], Vf[kb][s], wv, cat(Wl[u & 3][1], Wh[u & 3][1]));
        else if constexpr (i == 4) mma0(X[u & 1][kb], Vf[kb][s], wv);
        else if constexpr (i == 2) mma(X[u & 1][kb], Vf[kb][s], wv, cat(Wl[u & 3][3], Wh[u & 3][3]));
        else mma(X[u & 1][kb], Vf[kb][s], wv);
        SB();
        constexpr bool next_new = ((u + 1) & 3) == 0 || u == 61, after_new = ((u + 2) & 3) == 0 || u == 60;
        if constexpr (next_new && u < 63) {
          if constexpr (i == 1) {
            if constexpr (u == 3) waitv<0>();
            barl();
            STAMP(u == 61 ? 36 : 20 + ((u + 1) >> 2));
          }
          constexpr int lo[8] = {0, 0, 2, 4, 5, 6, 8, 9}, n[8] = {0, 2, 2, 1, 1, 2, 1, 1};
          static_for<0, n[i]>([&](auto k) { rdW(IC<8 * (u + 1) + lo[i] + k.value>{}); });
        } else if constexpr (rho < 512 && !(after_new && i >= 6)) rdW(IC<rho>{});
        if constexpr (i == 7) cvt(uu, IC<0>{});
        if constexpr (u >= 1 && (i == 0 || i == 3 || i == 4)) cvt(IC<(u >= 1 ? u - 1 : 0)>{}, IC<(i == 0 ? 1 : (i - 1) & 3)>{});
        if constexpr (pr >= 0 && ((i & 3) == 1 || (i & 3) == 2)) rdP(IC<(pr >= 0 ? pr : 0)>{}, IC<((i >> 2) * 2 + (i & 3) - 1) & 3>{});
        if constexpr ((u & 15) == 15 && u < 63 && i >= 2) rdV(IC<((i - 2) >> 2) & 1>{}, IC<(i - 2) & 3>{});
        if constexpr ((u & 15) == 0 && u > 0 && i < 2) rdV(IC<1>{}, IC<(i + 2) & 3>{});
      });
      if constexpr (pr >= 0 && u < 63)
        static_for<0, 8>([&](auto ii) { constexpr int i = ii.value; SB(); pvm(IC<(pr >= 0 ? pr : 0)>{}, IC<((i >> 1) & 1)>{}, IC<(i >> 2)>{}, IC<(i & 1)>{}); SB(); });
    });
    static_for<0, 4>([&](auto ii) { constexpr int i = ii.value; SB(); pvm(IC<15>{}, IC<0>{}, IC<(i >> 1)>{}, IC<(i & 1)>{}); SB();
      if constexpr (i == 0) cvt(IC<63>{}, IC<1>{}); });
    asm volatile("s_nop 2"); SB();
    cvt(IC<63>{}, IC<2>{}); cvt(IC<63>{}, IC<3>{});
    SB(); asm volatile("s_nop 1");
    static_for<0, 4>([&](auto ii) { constexpr int i = ii.value; SB(); pvm(IC<15>{}, IC<1>{}, IC<(i >> 1)>{}, IC<(i & 1)>{}); SB(); });
    mfma_done(out[0][0], out[0][1], out[1][0], out[1][1]);
    const __amdgpu_buffer_rsrc_t ors = __builtin_amdgcn_make_buffer_rsrc(obase, 0, 31 * 4096 + 64, 0x00020000);
#pragma unroll
    for (int qb = 0; qb < 2; ++qb) {
      v2u s0 = __builtin_amdgcn_permlane16_swap(pk(out[0][qb][0], out[0][qb][1]), pk(out[1][qb][0], out[1][qb][1]), false, false);
      v2u s1 = __builtin_amdgcn_permlane16_swap(pk(out[0][qb][2], out[0][qb][3]), pk(out[1][qb][2], out[1][qb][3]), false, false);
      v4u o4 = {s0[0], s1[0], s0[1], s1[1]};
      __builtin_amdgcn_raw_buffer_store_b128(o4, ors, ooff + qb * 16 * 4096, 0, OAUX);
    }
  }
#endif
}

extern "C" void kernel_launch(void* const* d_in, const int* in_sizes, int n_in, void* d_out, int out_size, void* d_ws, size_t ws_size,
                              hipStream_t stream) {
  Args a;
  for (int b = 0; b < 2; ++b) {
    a.x[b] = (const char*)d_in[b * 5 + 0]; a.k[b] = (const char*)d_in[b * 5 + 1]; a.v[b] = (const char*)d_in[b * 5 + 2];
    a.wq[b] = (const char*)d_in[b * 5 + 3]; a.wo[b] = (const char*)d_in[b * 5 + 4];
  }
  a.out = (char*)d_out; a.ws = (char*)d_ws;
  (void)hipFuncSetAttribute((const void*)gqa_fused_kernel, hipFuncAttributeMaxDynamicSharedMemorySize, LDS_BYTES);
  gqa_fused_kernel<<<256, 512, LDS_BYTES, stream>>>(a);
}
```

```cpp
#include <hip/hip_runtime.h>
#include <hip/hip_bf16.h>
#include <stdint.h>

typedef unsigned v4u __attribute__((ext_vector_type(4)));
typedef unsigned v2u __attribute__((ext_vector_type(2)));
typedef short s4 __attribute__((ext_vector_type(4)));
typedef short s8 __attribute__((ext_vector_type(8)));
typedef __bf16 bf8 __attribute__((ext_vector_type(8)));
typedef __bf16 bf2 __attribute__((ext_vector_type(2)));
typedef float f4v __attribute__((ext_vector_type(4)));
typedef float f2v __attribute__((ext_vector_type(2)));
#define LAS __attribute__((address_space(3)))
#define GAS __attribute__((address_space(1)))
#define DI __device__ __forceinline__
#define SB() __builtin_amdgcn_sched_barrier(0)

#define CPRIO 1
#define LPRIO 0
#define OAUX 0
#define PMODE 0
#define EARLYV 1
#define NTT 0
#define NTW 0
#define LT 7
#define TOUCH0 15
#define STAG1 13
#define STAG2 4

constexpr unsigned L_KP = 0;
constexpr unsigned L_RING = 32768;
constexpr unsigned L_V = 131072;
constexpr unsigned L_M = 147456;
constexpr unsigned L_X = L_RING;
constexpr unsigned LDS_BYTES = 163840;
constexpr size_t WS_N = 0, WS_I = 4096, WS_P = 65536;

template <int N> struct IC { static constexpr int value = N; };
template <int I, int N, class F> DI void static_for(F&& f) { if constexpr (I < N) { f(IC<I>{}); static_for<I + 1, N>(f); } }

#define GL4(P) "global_load_lds_dwordx4 %0, %4" P "\n\tglobal_load_lds_dwordx4 %1, %4 offset:1024" P "\n\t" \
               "global_load_lds_dwordx4 %2, %4 offset:2048" P "\n\tglobal_load_lds_dwordx4 %3, %4 offset:3072" P
struct Piece { unsigned v0, v1, v2, v3; const char* sb; unsigned lds; };
#define DMA_OPS "v"(p.v0), "v"(p.v1), "v"(p.v2), "v"(p.v3), "s"(p.sb), "s"(p.lds)
template <int POL> DI void dma4(const Piece& p) {
  if constexpr (POL == 1) asm volatile("s_mov_b32 m0, %5\n\ts_nop 0\n\t" GL4(" sc1") :: DMA_OPS : "memory");
  else if constexpr (POL == 2) asm volatile("s_mov_b32 m0, %5\n\ts_nop 0\n\t" GL4(" nt") :: DMA_OPS : "memory");
  else asm volatile("s_mov_b32 m0, %5\n\ts_nop 0\n\t" GL4("") :: DMA_OPS : "memory");
}
template <int N, int POL> DI void wb_dma4(const Piece& p) {
  if constexpr (POL == 1) asm volatile("s_mov_b32 m0, %5\n\ts_waitcnt vmcnt(%6)\n\ts_barrier\n\t" GL4(" sc1") :: DMA_OPS, "i"(N) : "memory");
  else if constexpr (POL == 2) asm volatile("s_mov_b32 m0, %5\n\ts_waitcnt vmcnt(%6)\n\ts_barrier\n\t" GL4(" nt") :: DMA_OPS, "i"(N) : "memory");
  else asm volatile("s_mov_b32 m0, %5\n\ts_waitcnt vmcnt(%6)\n\ts_barrier\n\t" GL4("") :: DMA_OPS, "i"(N) : "memory");
}
template <int N> DI void wb_dma4_sel(bool nt, const Piece& p) { if (nt) wb_dma4<N, 2>(p); else wb_dma4<N, 0>(p); }
DI void dma4_sel(bool nt, const Piece& p) { if (nt) dma4<2>(p); else dma4<0>(p); }
DI void touch(const char* p, unsigned lds) {
  asm volatile("s_mov_b32 m0, %1\n\ts_nop 0\n\tglobal_load_lds_dword %0, off" :: "v"(p), "s"(lds) : "memory");
}
DI void acquire() { if (PMODE == 1) asm volatile("buffer_inv sc1" ::: "memory"); }
template <int N> DI void waitv() {
 asm volatile("s_waitcnt vmcnt(%0)" ::"i"(N) : "memory"); }
DI void bar() { asm volatile("s_barrier" ::: "memory"); }
DI void barl() { asm volatile("s_waitcnt lgkmcnt(0)\n\ts_barrier" ::: "memory"); }
DI s4 tr(unsigned a) { return __builtin_amdgcn_ds_read_tr16_b64_v4i16((LAS s4*)(size_t)a); }
DI s8 cat(s4 lo, s4 hi) { return __builtin_shufflevector(lo, hi, 0, 1, 2, 3, 4, 5, 6, 7); }
DI s8 rd16(unsigned a) { return *(LAS s8*)(size_t)a; }
DI s4 rd8(unsigned a) { return *(LAS s4*)(size_t)a; }
DI f4v mfma(s8 a, s8 b, f4v c) { return __builtin_amdgcn_mfma_f32_16x16x32_bf16(__builtin_bit_cast(bf8, a), __builtin_bit_cast(bf8, b), c, 0, 0, 0); }
#define MF "v_mfma_f32_16x16x32_bf16 %0, %1, %2, "
DI void mma(f4v& c, s8 a, s8 b) { asm volatile(MF "%0" : "+v"(c) : "v"(a), "v"(b)); }
DI void mma(f4v& c, s8 a, s8 b, s8 e0) { asm volatile(MF "%0" : "+v"(c) : "v"(a), "v"(b), "v"(e0)); }
DI void mma(f4v& c, s8 a, s8 b, s8 e0, s8 e1, s8 e2, s8 e3) { asm volatile(MF "%0" : "+v"(c) : "v"(a), "v"(b), "v"(e0), "v"(e1), "v"(e2), "v"(e3)); }
DI void mma(f4v& c, s8 a, s8 b, s8 e0, s8 e1) { asm volatile(MF "%0" : "+v"(c) : "v"(a), "v"(b), "v"(e0), "v"(e1)); }
DI void mma(f4v& c, s8 a, s8 b, s8 e0, s8 e1, s8 e2) { asm volatile(MF "%0" : "+v"(c) : "v"(a), "v"(b), "v"(e0), "v"(e1), "v"(e2)); }
DI void mma0(f4v& c, s8 a, s8 b) { asm volatile(MF "0" : "=&v"(c) : "v"(a), "v"(b)); }
DI void mma0(f4v& c, s8 a, s8 b, s8 e0) { asm volatile(MF "0" : "=&v"(c) : "v"(a), "v"(b), "v"(e0)); }
DI void mfma_done(f4v& a, f4v& b, f4v& c, f4v& d) { asm volatile("s_nop 7\n\ts_nop 1" : "+v"(a), "+v"(b), "+v"(c), "+v"(d)); }
DI void pin4(f4v& a, f4v& b, f4v& c, f4v& d) { asm volatile("" : "+v"(a), "+v"(b), "+v"(c), "+v"(d)); }
DI unsigned pk(float a, float b) { f2v v = {a, b}; return __builtin_bit_cast(unsigned, __builtin_convertvector(v, bf2)); }
DI s8 pack8(f4v a, f4v b) { v4u r = {pk(a[0], a[1]), pk(a[2], a[3]), pk(b[0], b[1]), pk(b[2], b[3])}; return __builtin_bit_cast(s8, r); }
DI unsigned opaque(unsigned x) { asm volatile("" : "+v"(x)); return x; }
DI unsigned vmax(unsigned a, unsigned b) { unsigned r; asm("v_max_f32 %0, %1, %2" : "=v"(r) : "v"(a), "v"(b)); return r; }
constexpr unsigned slot_off(int n) { return (unsigned)(n % 3) * 32768u; }
constexpr int pv_unit(int p) { const int e = 4 * (LT + (p >> 1)) + 2 * (p & 1); return e > 4 * p + 4 ? e : 4 * p + 4; }
constexpr int pair_at(int u) { for (int p = 0; p < 16; ++p) if (pv_unit(p) == u || (u == 63 && p == 15)) return p; return -1; }
constexpr int p_issue(int m) { const int a = LT + m - 1, b = m >= 2 ? (pv_unit(2 * m - 3) + 1) / 4 + 1 : 0; return a > b ? a : b; }
constexpr int p_at(int j) { for (int m = 0; m < 8; ++m) if (p_issue(m) == j) return m; return -1; }
constexpr bool pv_ok() {
  for (int m = 0; m < 8; ++m) if (4 * (p_issue(m) + 1) > pv_unit(2 * m) || (m > 0 && p_issue(m) <= p_issue(m - 1))) return false;
  return pv_unit(14) < 63 && pv_unit(15) == 64 && p_issue(7) <= 13 && LT >= 4; }
static_assert(pv_ok(), "P schedule");

#define STAMP(i)

struct Args {
  const char* x[2]; const char* k[2]; const char* v[2]; const char* wq[2]; const char* wo[2];
  char* out; char* ws;
};

__global__ __launch_bounds__(512, 2) void gqa_fused_kernel(Args a) {
#if __HIP_DEVICE_COMPILE__
  const int lane = threadIdx.x & 63;
  const int w = __builtin_amdgcn_readfirstlane(threadIdx.x >> 6);
  const int xcd = blockIdx.x & 7, idx = blockIdx.x >> 3;
  const int br = xcd >> 2, hg = xcd & 3, i8 = idx & 7, rb = idx >> 3;
  const int head = hg * 8 + i8;
  const int ct = hg * 4 + (i8 & 3), rp = rb * 2 + (i8 >> 2);
  const int ord = STAG2 == 1 ? ((rp + 2 * ct) & 7) : STAG2 == 2 ? ((rp + 2 * ct) & 6) : 0;
  const int g0 = STAG2 >= 4 ? (ct + 2 * rp) & 3 : ord >> 1;
  const int rot2 = STAG2 >= 4 ? (rp >> 1) & 3 : (ord & 1) * 2;
  const bool late = STAG2 == 4 ? (ct & 2) != 0 : STAG2 == 6;
  auto grp = [&](int k) { return (g0 + (late ? ((k >> 1) | ((k & 1) << 1)) : k)) & 3; };
  const int kt0 = STAG1 == 3 ? 8 * ((rb ^ i8) & 1) : STAG1 == 4 ? 8 * ((rb ^ i8) & 1) + 2 * hg : STAG1 == 10 || STAG1 == 13 ? 4 * hg : STAG1 == 12 ? 2 * hg : 0;
  const int rot1 = STAG1 == 13 ? rb + (i8 & 1) + (i8 >> 2) * 2 : STAG1 >= 9 ? rb + i8 : 0;
  const bool shift1 = STAG1 == 13 ? (i8 & 2) != 0 : (STAG1 == 9 || STAG1 == 10 || STAG1 == 12) && i8 >= 4;
  auto ktile = [&](int s) {
    const int plain = STAG1 >= 9 ? (s & 12) + ((s + rot1) & 3) : s;
    const int shifted = (s < 2 || s >= 14) ? (s & 14) + ((s + rot1) & 1) : 2 + ((s - 2) & 12) + ((s - 2 + rot1) & 3);
    return ((shift1 ? shifted : plain) + kt0) & 15; };
  char* const wsP = a.ws + WS_P + (size_t)br * (16 * 32 * 2048);

  if (w >= 4) {
    const int lw = w - 4;
    __builtin_amdgcn_s_setprio(LPRIO);
    const char* const x = br ? a.x[1] : a.x[0];
    const char* const wq = br ? a.wq[1] : a.wq[0];
    const char* const kc = br ? a.k[1] : a.k[0];
    const char* const vc = br ? a.v[1] : a.v[0];
    const char* const wo = br ? a.wo[1] : a.wo[0];
    unsigned vo[8], vk[8], vw[4];
    const char* sb; size_t tstride;
    if (lw < 2) {
#pragma unroll
      for (int i = 0; i < 8; ++i) { const int row = lw * 64 + i * 8 + (lane >> 3); vo[i] = row * 2048 + (((lane & 7) ^ ((row >> 1) & 7)) << 4) + 4096 - (i & 3) * 1024; }
      sb = x + (size_t)rb * 128 * 2048 - 4096; tstride = 128;
    } else {
#pragma unroll
      for (int i = 0; i < 8; ++i) { const int k = (lw - 2) * 32 + i * 4 + (lane >> 4); const int f = ((k & 3) | (((k >> 3) & 1) << 2)) << 1;
        vo[i] = k * 8192 + (((lane & 15) ^ f) << 4) + 4096 - (i & 3) * 1024; }
      sb = wq + head * 256 - 4096; tstride = 64 * 8192;
    }
    auto ntT = [&](int t) { return NTT && (NTT == 3 || lw >= 2) && (t == 0 || (t & 3) == (shift1 ? 2 : 0)); };
    auto ntW = [&](int j) { return NTW && (j == 0 || j == (late ? 8 : 4)); };
    auto pT = [&](int t, int half) { return Piece{vo[4 * half], vo[4 * half + 1], vo[4 * half + 2], vo[4 * half + 3], sb + ktile(t) * tstride,
                                                  L_RING + slot_off(t) + lw * 8192 + half * 4096}; };
    dma4_sel(ntT(0), pT(0, 0)); dma4_sel(ntT(0), pT(0, 1));
    dma4<0>(pT(1, 0)); dma4<0>(pT(1, 1));
#pragma unroll
    for (int i = 0; i < 8; ++i) { const int row = i * 4 + (lane >> 4); vk[i] = row * 256 + (((lane & 15) ^ (row & 15)) << 4) + 4096 - (i & 3) * 1024; }
#pragma unroll
    for (int i = 0; i < 4; ++i) { const int f = ((lane >> 4) & 1) | ((i & 1) << 1);
      vw[i] = (lw * 32 + i * 8 + (lane >> 3)) * 2048 + (((lane & 7) ^ (f << 1)) << 4) + 4096 - i * 1024; }
    const unsigned vv0 = (lw & 1) ? vk[4] : vk[0], vv1 = (lw & 1) ? vk[5] : vk[1], vv2 = (lw & 1) ? vk[6] : vk[2], vv3 = (lw & 1) ? vk[7] : vk[3];
    const unsigned vp = lane * 16;
    auto pK = [&](int half) { return Piece{vk[4 * half], vk[4 * half + 1], vk[4 * half + 2], vk[4 * half + 3],
                                           kc + (size_t)((rb * 4 + lw) * 4 + hg) * 8192 - 4096, L_KP + lw * 8192 + half * 4096}; };
    auto pW = [&](int j, int half) { const int hp = grp(j >> 2) * 4 + ((j + rot2) & 3);
      return Piece{vw[0], vw[1], vw[2], vw[3], wo + (size_t)(hp * 2 + half) * 128 * 2048 + ct * 128 - 4096, L_RING + slot_off(16 + j) + half * 16384 + lw * 4096}; };
    auto pV = [&](int g) { const int gg = grp(g);
      return Piece{vv0, vv1, vv2, vv3, vc + (size_t)((rp * 2 + (lw >> 1)) * 4 + gg) * 8192 - 4096, L_V + lw * 4096}; };
    auto pP = [&](int m) { const int hp = grp(m >> 1) * 4 + ((2 * m + (lw & 1) + rot2) & 3);
      return Piece{vp, vp, vp, vp, wsP + (size_t)((rp * 2 + (lw >> 1)) * 32 + hp * 2) * 2048, L_KP + (m & 1) * 16384 + lw * 4096}; };
    GAS unsigned* const gN = (GAS unsigned*)(a.ws + WS_N) + (br * 4 + rb) * 32;
    GAS unsigned* const gI = (GAS unsigned*)(a.ws + WS_I) + (br * 4 + rb) * 1024;
    GAS unsigned* const myN = gN + (lane & 31);
    GAS unsigned* const myI = gI + head * 32 + (lane & 31);
    GAS unsigned* const outI = gI + (lane & 31) * 32 + head;
    unsigned entry, poll0, nfin, nonce, nused;
    auto aload = [&](unsigned& d, GAS unsigned* p) { asm volatile("global_load_dword %0, %1, off sc1" : "=v"(d) : "v"(p) : "memory"); };
    auto landed = [&](unsigned& d) { asm volatile("" : "+v"(d)); };
    auto astore = [&](GAS unsigned* p, unsigned v) { asm volatile("global_store_dword %0, %1, off sc1" :: "v"(p), "v"(v) : "memory"); };
    const int slowc = (3 - (int)((size_t)wo >> 7)) & 7;
    const bool towner = (slowc >> 2) == (hg & 1);
    const int tpiece = TOUCH0 >= 14 ? (rb * 2 + (i8 >> 2)) * 4 + lw : idx;
    const int tstep = TOUCH0 >= 14 ? (towner && (i8 & 3) == 0 ? (TOUCH0 == 14 ? 6 : lw < 2 ? 6 : 10) : -1) : (TOUCH0 > 0 && lw == 0 && towner) ? TOUCH0 + (idx >> 2) : -1;
    const int thp = STAG2 >= 4 ? ((slowc + 1 + 2 * (tpiece >> 4)) & 3) * 4 + ((tpiece >> 2) & 3) : (tpiece >> 3) * 4 + 2 + ((tpiece >> 2) & 1);
    const char* const tptr = wo + (size_t)((thp * 256 + (tpiece & 3) * 64 + lane) * 2048) + (hg * 4 + (slowc & 3)) * 128;
    const char* const kptr = kc + (size_t)((rb * 4 + lw) * 4 + hg) * 8192 + (i8 * 8 + (lane & 7)) * 128;
    static_for<0, 14>([&](auto nn) {
      constexpr int n = nn.value;
      if constexpr (EARLYV && n >= 9 && n <= 12) { if (shift1 == (n <= 10)) wb_dma4_sel<13>(ntT(n + 2), pT(n + 2, 0)); else wb_dma4_sel<8>(ntT(n + 2), pT(n + 2, 0)); }
      else wb_dma4_sel<(n == 1 ? 9 : 8)>(ntT(n + 2), pT(n + 2, 0));
      dma4_sel(ntT(n + 2), pT(n + 2, 1));
      if constexpr (EARLYV && (n == 8 || n == 10)) { if (shift1 == (n == 8)) { touch(kptr, L_M + 2048 + lw * 256); dma4<0>(pV(0)); } }
      if constexpr (n == 0) aload(entry, myI);
      if constexpr (n == 2) {
        landed(entry);
        if (lw == 0) {
          unsigned long long live = 0xffffffffull; unsigned nv = 0;
#pragma unroll
          for (int b = 0; b < 6; ++b) {
            const unsigned long long one = __builtin_amdgcn_ballot_w64((entry >> b) & 1u) & live, zero = live & ~one;
            const bool pick1 = __builtin_popcountll(one) < __builtin_popcountll(zero);
            nv |= pick1 ? (1u << b) : 0u; live = pick1 ? one : zero;
          }
          astore(gN + head, nv);
          *(volatile LAS unsigned*)(size_t)(L_M + 16) = nv;
          asm volatile("s_waitcnt lgkmcnt(0)" ::: "memory");
        }
      }
      if constexpr (n >= 2) { if (n == tstep) touch(tptr, L_M + 1024 + lw * 256); }
      if constexpr (n == 4) nonce = opaque(*(volatile LAS unsigned*)(size_t)(L_M + 16));
    });
    wb_dma4<8, 0>(pK(0)); dma4<0>(pK(1));
    wb_dma4_sel<8>(ntW(0), pW(0, 0)); dma4_sel(ntW(0), pW(0, 1));
    aload(nused, myN);
    if constexpr (EARLYV) wb_dma4<9, 0>(pW(1, 0)); else { wb_dma4<9, 0>(pV(0)); dma4<0>(pW(1, 0)); }
    dma4<0>(pW(1, 1));
    wb_dma4<8, 0>(pW(2, 0)); dma4<0>(pW(2, 1));
    static_for<1, 16>([&](auto jj) {
      constexpr int j = jj.value;
      constexpr int m = p_at(j);
      constexpr bool hasP = m >= 0, hasV = j % 4 == 1 && j <= 9, hasW = j + 2 <= 15;
      constexpr int N = j == 15 ? 4 : j == LT + 1 ? 9 : 8;
      auto rest = [&](auto skip) {
        if constexpr (hasP && skip.value != 1) dma4<PMODE == 0>(pP(m));
        if constexpr (hasV && skip.value != 2) dma4<0>(pV(j / 4 + 1));
        if constexpr (hasW) { if constexpr (skip.value != 3) dma4_sel(ntW(j + 2), pW(j + 2, 0)); dma4_sel(ntW(j + 2), pW(j + 2, 1)); }
      };
      if constexpr (j == 1) {
        waitv<N>(); bar(); landed(nused);
        if (lw == 0) astore(outI, nused);
        STAMP(37);
        rest(IC<0>{});
      } else if constexpr (j == LT - 2) {
        waitv<N>(); bar(); aload(poll0, myI); rest(IC<0>{});
      } else if constexpr (j == LT - 1) {
        waitv<N>(); bar(); landed(poll0);
        if (__builtin_amdgcn_ballot_w64(poll0 != nonce) == 0) { STAMP(38); acquire(); rest(IC<0>{}); }
        else {
          rest(IC<1>{});
          _Pragma("unroll 1") for (unsigned spins = 0; spins < (1u << 20); ++spins) {
            unsigned f, nn2;
            asm volatile("global_load_dword %0, %2, off sc1\n\tglobal_load_dword %1, %3, off sc1\n\ts_waitcnt vmcnt(0)"
                         : "=&v"(f), "=&v"(nn2) : "v"(myI), "v"(myN) : "memory");
            if (__builtin_amdgcn_ballot_w64(f != nonce) == 0) break;
            if (lw == 0 && __builtin_amdgcn_ballot_w64(nn2 != nused) != 0) { nused = nn2; astore(outI, nused); }
            __builtin_amdgcn_s_sleep(2);
          }
          STAMP(38);
          acquire(); dma4<PMODE == 0>(pP(0)); waitv<0>();
        }
      } else if constexpr (hasP) { wb_dma4<N, PMODE == 0>(pP(m)); rest(IC<1>{}); }
      else if constexpr (hasV) { wb_dma4<N, 0>(pV(j / 4 + 1)); rest(IC<2>{}); }
      else if constexpr (hasW) { wb_dma4_sel<N>(ntW(j + 2), pW(j + 2, 0)); rest(IC<3>{}); }
      else { waitv<N>(); bar(); }
      if constexpr (j == LT) aload(nfin, myN);
      if constexpr (j == LT + 2) { landed(nfin); if (lw == 0 && __builtin_amdgcn_ballot_w64(nfin != nused) != 0) astore(outI, nfin); }
    });
    waitv<0>(); bar();
  } else {
    STAMP(0);
    __builtin_amdgcn_s_setprio(CPRIO);
    const int g = lane >> 4, q4 = (lane & 15) >> 2, p = lane & 3, r = lane & 15;
    const int rh = w >> 1, dh = w & 1;
    s8 Pfrag[2];
    char* pbase = wsP + (size_t)((rb * 4 + w) * 32 + head) * 2048;
    char* obase = a.out + (size_t)((rp * 2 + (w >> 1)) * 32) * 4096 + br * 2048 + ct * 128 + (w & 1) * 64;
    asm volatile("" : "+s"(pbase), "+s"(obase));
    const unsigned ooff = opaque(r * 4096 + ((g & 1) * 16 + (g >> 1) * 8) * 2);
    const int b2 = w >> 1, c2 = w & 1;
    unsigned aW[2][2];
#pragma unroll
    for (int nb = 0; nb < 2; ++nb) { aW[0][nb] = opaque(L_RING + (8 * g + q4) * 128 + (((c2 * 2 + nb) ^ ((q4 >> 1) | ((g & 1) << 1))) << 5) + p * 8); aW[1][nb] = opaque(aW[0][nb] + 65536); }
    const unsigned aP = opaque(L_KP + b2 * 8192 + lane * 16);
    unsigned aV[4];
#pragma unroll
    for (int s = 0; s < 4; ++s) aV[s] = opaque(L_V + (b2 * 32 + r) * 256 + (((4 * s + g) ^ r) << 4));
    {
      unsigned aA[2][4], aB[2][2][2];
#pragma unroll
      for (int c = 0; c < 4; ++c) { aA[0][c] = opaque(L_RING + 16384 + (8 * g + q4) * 256 + (((dh * 4 + c) ^ (q4 | ((g & 1) << 2))) << 5) + p * 8); aA[1][c] = opaque(aA[0][c] + 65536); }
#pragma unroll
      for (int kk = 0; kk < 2; ++kk)
#pragma unroll
        for (int o = 0; o < 2; ++o) { const int row = rh * 64 + ((o ^ dh ^ 1) << 5) + r;
          aB[0][kk][o] = opaque(L_RING + row * 128 + (((4 * kk + g) ^ ((row >> 1) & 7)) << 4)); aB[1][kk][o] = opaque(aB[0][kk][o] + 65536); }
      f4v acc[4][4];
      s4 Al[4][2], Ah[4][2]; s8 B[2][4][2];
      auto rdA = [&](auto tile, auto cc, auto ii) { constexpr int c = cc.value, kk = ii.value >> 1;
        constexpr unsigned so = slot_off(tile.value); constexpr int hi = so >= 65536; constexpr unsigned o = so - hi * 65536 + kk * 8192;
        if constexpr (ii.value & 1) Ah[c][kk] = tr(aA[hi][c] + o + 1024); else Al[c][kk] = tr(aA[hi][c] + o); };
      auto rdB = [&](auto tile, auto bb, auto kk) { constexpr int b = bb.value;
        constexpr unsigned so = slot_off(tile.value); constexpr int hi = so >= 65536; constexpr unsigned o = so - hi * 65536;
        B[tile.value & 1][b][kk.value] = rd16(aB[hi][kk.value][b >> 1] + o + (b & 1) * 2048); };
      auto rdNext = [&](auto tile, auto ii) { constexpr int i = ii.value;
        if constexpr (i < 2) rdB(tile, IC<0>{}, ii); else if constexpr (i < 6) rdA(tile, IC<0>{}, IC<i - 2>{});
        else rdB(tile, IC<(i - 4) / 2>{}, IC<i & 1>{}); };
      auto mm1 = [&](auto tile, auto cc, auto bb, auto kc) { constexpr int t = tile.value, c = cc.value, b = bb.value, kk = kc.value;
        const s8 av = cat(Al[c][kk], Ah[c][kk]), bv = B[t & 1][b][kk];
        if constexpr (t == 0 && kk == 0) mma0(acc[c][b], av, bv);
        else if constexpr (kk == 0 && c == 0 && b == 0) mma(acc[c][b], av, bv, cat(Al[0][1], Ah[0][1]), B[t & 1][1][1]);
        else if constexpr (kk == 0 && c == 0 && b == 2) mma(acc[c][b], av, bv, B[t & 1][3][1]);
        else if constexpr (kk == 0 && c == 1 && b == 0) mma(acc[c][b], av, bv, cat(Al[1][1], Ah[1][1]));
        else if constexpr (kk == 0 && c == 2 && b == 0) mma(acc[c][b], av, bv, cat(Al[2][1], Ah[2][1]), cat(Al[3][0], Ah[3][0]), cat(Al[3][1], Ah[3][1]));
        else mma(acc[c][b], av, bv); };
      unsigned aK[2];
#pragma unroll
      for (int o = 0; o < 2; ++o) aK[o] = opaque(L_KP + ((rh * 2 + (o ^ dh ^ 1)) * 32 + r) * 256 + (g & 1) * 8 + (((dh * 8 + (g >> 1)) ^ r) << 4));
      unsigned aK2[2][3];
#pragma unroll
      for (int o = 0; o < 2; ++o)
#pragma unroll
        for (int i = 0; i < 3; ++i) aK2[o][i] = opaque(aK[o] ^ ((i + 1) << 5));
      const unsigned aXw = opaque(L_X + w * 4096 + lane * 16), aXr = opaque(L_X + (w ^ 1) * 4096 + lane * 16);
      const unsigned aFw = opaque(L_M + w * 4), aFr = opaque(L_M + (w ^ 1) * 4);
      *(volatile LAS unsigned*)(size_t)aFw = 0u;
      barl();
      STAMP(1);
      static_for<0, 12>([&](auto ii) { rdNext(IC<0>{}, ii); });
      static_for<0, 16>([&](auto t) {
        static_for<0, 16>([&](auto mm) { constexpr int m = mm.value;
          SB(); mm1(t, IC<(m >> 3)>{}, IC<((m >> 1) & 3)>{}, IC<(m & 1)>{}); SB();
          if constexpr (m < 12) rdA(t, IC<1 + (m >> 2)>{}, IC<(m & 3)>{}); });
        SB(); barl();
        STAMP(2 + t.value);
        if constexpr (t.value < 15) static_for<0, 16>([&](auto mm) { constexpr int m = mm.value;
          SB(); mm1(t, IC<2 + (m >> 3)>{}, IC<((m >> 1) & 3)>{}, IC<(m & 1)>{}); SB();
          if constexpr (m < 12) rdNext(IC<t.value + 1>{}, mm); });
      });
      s4 Kh[2][2][2][2];
      unsigned q[4][2][4];
      f4v St[2][2][2];
      auto rdK = [&](auto ii) { constexpr int i = ii.value, o = i >> 3, kb = (i >> 2) & 1, s = (i >> 1) & 1, u = i & 1, x = s * 2 + u;
        Kh[o][kb][s][u] = rd8((x == 0 ? aK[o] : aK2[o][x - 1]) + kb * 4096); };
      auto cvq = [&](auto bb, auto cc) { constexpr int b = bb.value, c = cc.value;
        q[b][c >> 1][(c & 1) * 2] = opaque(pk(acc[c][b][0], acc[c][b][1])); q[b][c >> 1][(c & 1) * 2 + 1] = opaque(pk(acc[c][b][2], acc[c][b][3])); };
      auto smm = [&](auto ii) { constexpr int i = ii.value, o = i >> 3, qb = (i >> 2) & 1, kb = (i >> 1) & 1, s = i & 1;
        const v4u qq = {q[2 * o + qb][s][0], q[2 * o + qb][s][1], q[2 * o + qb][s][2], q[2 * o + qb][s][3]};
        if constexpr (s == 0) mma0(St[o][kb][qb], cat(Kh[o][kb][s][0], Kh[o][kb][s][1]), __builtin_bit_cast(s8, qq));
        else mma(St[o][kb][qb], cat(Kh[o][kb][s][0], Kh[o][kb][s][1]), __builtin_bit_cast(s8, qq)); };
      static_for<0, 16>([&](auto mm) {
        constexpr int m = mm.value;
        constexpr int cb[15] = {0, 0, 1, 1, 0, 0, 2, 2, 3, 3, 1, 1, -1, 2, 2}, cc[15] = {0, 1, 0, 1, 2, 3, 0, 1, 0, 1, 2, 3, -1, 2, 3};
        SB(); mm1(IC<15>{}, IC<2 + ((m >> 1) & 1)>{}, IC<(m >> 2)>{}, IC<(m & 1)>{}); SB();
        rdK(mm);
        if constexpr (m < 15) { if constexpr (cb[m] >= 0) cvq(IC<cb[m]>{}, IC<cc[m]>{}); }
      });
      static_for<0, 16>([&](auto mm) {
        constexpr int m = mm.value;
        SB(); smm(mm); SB();
        if constexpr (m == 4) cvq(IC<3>{}, IC<2>{});
        if constexpr (m == 5) cvq(IC<3>{}, IC<3>{});
        if constexpr (m == 9 || m == 10 || m == 12 || m == 14) { constexpr int t = m == 9 ? 0 : m == 10 ? 1 : m == 12 ? 2 : 3;
          *(LAS f4v*)(size_t)(aXw + t * 1024) = St[0][t & 1][t >> 1]; }
      });
      SB();
      asm volatile("s_waitcnt lgkmcnt(0)" ::: "memory");
      *(volatile LAS unsigned*)(size_t)aFw = 1u;
      while (__builtin_amdgcn_readfirstlane(*(volatile LAS unsigned*)(size_t)aFr) == 0u) {}
      asm volatile("" ::: "memory");
      STAMP(18);
      pin4(St[1][0][0], St[1][0][1], St[1][1][0], St[1][1][1]);
#pragma unroll
      for (int t = 0; t < 4; ++t) St[1][t & 1][t >> 1] += *(LAS f4v*)(size_t)(aXr + t * 1024);
      const float cs = 0.08838834764831845f * 1.4426950408889634f;
      f4v e[2][2];
      float m2[2];
#pragma unroll
      for (int qb = 0; qb < 2; ++qb) { const f4v u = St[1][0][qb], v = St[1][1][qb];
        m2[qb] = fmaxf(fmaxf(fmaxf(fmaxf(u[0], u[1]), u[2]), fmaxf(fmaxf(u[3], v[0]), v[1])), fmaxf(v[2], v[3])); }
      v2u t0 = __builtin_amdgcn_permlane16_swap(__float_as_uint(m2[0]), __float_as_uint(m2[1]), false, false);
      unsigned z = vmax(t0[0], t0[1]);
      t0 = __builtin_amdgcn_permlane32_swap(z, z, false, false);
      z = __float_as_uint(__uint_as_float(vmax(t0[0], t0[1])) * -cs);
      t0 = __builtin_amdgcn_permlane16_swap(z, z, false, false);
      float s2[2];
#pragma unroll
      for (int qb = 0; qb < 2; ++qb) {
        const float mc = __uint_as_float(t0[qb]);
#pragma unroll
        for (int kb = 0; kb < 2; ++kb) {
          const f4v y = __builtin_elementwise_fma(St[1][kb][qb], (f4v){cs, cs, cs, cs}, (f4v){mc, mc, mc, mc});
#pragma unroll
          for (int i = 0; i < 4; ++i) e[qb][kb][i] = __builtin_amdgcn_exp2f(y[i]);
        }
        const f4v sm = e[qb][0] + e[qb][1];
        s2[qb] = (sm[0] + sm[2]) + (sm[1] + sm[3]);
      }
      t0 = __builtin_amdgcn_permlane16_swap(__float_as_uint(s2[0]), __float_as_uint(s2[1]), false, false);
      z = __float_as_uint(__uint_as_float(t0[0]) + __uint_as_float(t0[1]));
      t0 = __builtin_amdgcn_permlane32_swap(z, z, false, false);
      z = __float_as_uint(__builtin_amdgcn_rcpf(__uint_as_float(t0[0]) + __uint_as_float(t0[1])));
      t0 = __builtin_amdgcn_permlane16_swap(z, z, false, false);
#pragma unroll
      for (int qb = 0; qb < 2; ++qb) { const float rs = __uint_as_float(t0[qb]); Pfrag[qb] = pack8(e[qb][0] * rs, e[qb][1] * rs); }
    }
    {
      const __amdgpu_buffer_rsrc_t rs = __builtin_amdgcn_make_buffer_rsrc(pbase, 0, 2048, 0x00020000);
      __builtin_amdgcn_raw_buffer_store_b128(__builtin_bit_cast(v4u, Pfrag[0]), rs, lane * 16, 0, 16);
      __builtin_amdgcn_raw_buffer_store_b128(__builtin_bit_cast(v4u, Pfrag[1]), rs, lane * 16 + 1024, 0, 16);
    }
    s8 Vf[2][4];
    s4 Wl[4][4], Wh[4][4];
    f4v X[2][2];
    unsigned Xb[32][2][4];
    s8 Pf[2][2];
    f4v out[2][2];
    auto rdW = [&](auto rr) { constexpr int rho = rr.value, u = rho >> 3, s = (rho >> 1) & 3;
      constexpr unsigned so = slot_off(16 + (u >> 2)); constexpr int hi = so >= 65536;
      constexpr unsigned o = so - hi * 65536 + ((u >> 1) & 1) * 16384 + s * 4096 + (rho & 1) * 512;
      if constexpr (rho & 1) Wh[u & 3][s] = tr(aW[hi][u & 1] + o); else Wl[u & 3][s] = tr(aW[hi][u & 1] + o); };
    auto rdV = [&](auto kb, auto s) { Vf[kb.value][s.value] = rd16(aV[s.value] + kb.value * 4096); };
    auto rdP = [&](auto pp, auto ii) { constexpr int h = 2 * pp.value + (ii.value & 1), qb = ii.value >> 1;
      Pf[h & 1][qb] = rd16(aP + ((h >> 2) & 1) * 16384 + (h & 3) * 2048 + qb * 1024); };
    auto cvt = [&](auto uu, auto ii) { constexpr int u = uu.value, i = ii.value;
      Xb[u >> 1][u & 1][i] = opaque(pk(X[u & 1][i >> 1][(i & 1) * 2], X[u & 1][i >> 1][(i & 1) * 2 + 1])); };
    auto pvm = [&](auto pp, auto nn, auto qq, auto ee) { constexpr int h = 2 * pp.value + ee.value, nb = nn.value, qb = qq.value;
      const v4u xa = {Xb[h][nb][0], Xb[h][nb][1], Xb[h][nb][2], Xb[h][nb][3]};
      if constexpr (h == 0) mma0(out[nb][qb], __builtin_bit_cast(s8, xa), Pf[h & 1][qb]);
      else if constexpr (nb == 0 && ee.value == 0) mma(out[nb][qb], __builtin_bit_cast(s8, xa), Pf[h & 1][qb], Pf[1][qb]);
      else mma(out[nb][qb], __builtin_bit_cast(s8, xa), Pf[h & 1][qb]); };
    STAMP(19);
    barl();
    STAMP(20);
    static_for<0, 4>([&](auto s) { rdW(IC<2 * s.value>{}); rdW(IC<2 * s.value + 1>{}); rdV(IC<0>{}, s); });
    static_for<0, 4>([&](auto s) { rdV(IC<1>{}, s); });
    rdW(IC<8>{}); rdW(IC<9>{});
    static_for<0, 64>([&](auto uu) {
      constexpr int u = uu.value, pr = pair_at(u);
      static_for<0, 8>([&](auto ii) {
        constexpr int i = ii.value, kb = i >> 2, s = i & 3, rho = 8 * u + i + 10;
        const s8 wv = cat(Wl[u & 3][s], Wh[u & 3][s]);
        SB();
        if constexpr (i == 0) mma0(X[u & 1][kb], Vf[kb][s], wv, cat(Wl[u & 3][1], Wh[u & 3][1]));
        else if constexpr (i == 4) mma0(X[u & 1][kb], Vf[kb][s], wv);
        else if constexpr (i == 2) mma(X[u & 1][kb], Vf[kb][s], wv, cat(Wl[u & 3][3], Wh[u & 3][3]));
        else mma(X[u & 1][kb], Vf[kb][s], wv);
        SB();
        constexpr bool next_new = ((u + 1) & 3) == 0 || u == 61, after_new = ((u + 2) & 3) == 0 || u == 60;
        if constexpr (next_new && u < 63) {
          if constexpr (i == 1) {
            if constexpr (u == 3) waitv<0>();
            barl();
            STAMP(u == 61 ? 36 : 20 + ((u + 1) >> 2));
          }
          constexpr int lo[8] = {0, 0, 2, 4, 5, 6, 8, 9}, n[8] = {0, 2, 2, 1, 1, 2, 1, 1};
          static_for<0, n[i]>([&](auto k) { rdW(IC<8 * (u + 1) + lo[i] + k.value>{}); });
        } else if constexpr (rho < 512 && !(after_new && i >= 6)) rdW(IC<rho>{});
        if constexpr (i == 7) cvt(uu, IC<0>{});
        if constexpr (u >= 1 && (i == 0 || i == 3 || i == 4)) cvt(IC<(u >= 1 ? u - 1 : 0)>{}, IC<(i == 0 ? 1 : (i - 1) & 3)>{});
        if constexpr (pr >= 0 && ((i & 3) == 1 || (i & 3) == 2)) rdP(IC<(pr >= 0 ? pr : 0)>{}, IC<((i >> 2) * 2 + (i & 3) - 1) & 3>{});
        if constexpr ((u & 15) == 15 && u < 63 && i >= 2) rdV(IC<((i - 2) >> 2) & 1>{}, IC<(i - 2) & 3>{});
        if constexpr ((u & 15) == 0 && u > 0 && i < 2) rdV(IC<1>{}, IC<(i + 2) & 3>{});
      });
      if constexpr (pr >= 0 && u < 63)
        static_for<0, 8>([&](auto ii) { constexpr int i = ii.value; SB(); pvm(IC<(pr >= 0 ? pr : 0)>{}, IC<((i >> 1) & 1)>{}, IC<(i >> 2)>{}, IC<(i & 1)>{}); SB(); });
    });
    static_for<0, 4>([&](auto ii) { constexpr int i = ii.value; SB(); pvm(IC<15>{}, IC<0>{}, IC<(i >> 1)>{}, IC<(i & 1)>{}); SB();
      if constexpr (i == 0) cvt(IC<63>{}, IC<1>{}); });
    asm volatile("s_nop 2"); SB();
    cvt(IC<63>{}, IC<2>{}); cvt(IC<63>{}, IC<3>{});
    SB(); asm volatile("s_nop 1");
    static_for<0, 4>([&](auto ii) { constexpr int i = ii.value; SB(); pvm(IC<15>{}, IC<1>{}, IC<(i >> 1)>{}, IC<(i & 1)>{}); SB(); });
    mfma_done(out[0][0], out[0][1], out[1][0], out[1][1]);
    const __amdgpu_buffer_rsrc_t ors = __builtin_amdgcn_make_buffer_rsrc(obase, 0, 31 * 4096 + 64, 0x00020000);
#pragma unroll
    for (int qb = 0; qb < 2; ++qb) {
      v2u s0 = __builtin_amdgcn_permlane16_swap(pk(out[0][qb][0], out[0][qb][1]), pk(out[1][qb][0], out[1][qb][1]), false, false);
      v2u s1 = __builtin_amdgcn_permlane16_swap(pk(out[0][qb][2], out[0][qb][3]), pk(out[1][qb][2], out[1][qb][3]), false, false);
      v4u o4 = {s0[0], s1[0], s0[1], s1[1]};
      __builtin_amdgcn_raw_buffer_store_b128(o4, ors, ooff + qb * 16 * 4096, 0, OAUX);
    }
  }
#endif
}

extern "C" void kernel_launch(void* const* d_in, const int* in_sizes, int n_in, void* d_out, int out_size, void* d_ws, size_t ws_size,
                              hipStream_t stream) {
  Args a;
  for (int b = 0; b < 2; ++b) {
    a.x[b] = (const char*)d_in[b * 5 + 0]; a.k[b] = (const char*)d_in[b * 5 + 1]; a.v[b] = (const char*)d_in[b * 5 + 2];
    a.wq[b] = (const char*)d_in[b * 5 + 3]; a.wo[b] = (const char*)d_in[b * 5 + 4];
  }
  a.out = (char*)d_out; a.ws = (char*)d_ws;
  (void)hipFuncSetAttribute((const void*)gqa_fused_kernel, hipFuncAttributeMaxDynamicSharedMemorySize, LDS_BYTES);
  gqa_fused_kernel<<<256, 512, LDS_BYTES, stream>>>(a);
}
```

```cpp
#include <hip/hip_runtime.h>
#include <hip/hip_bf16.h>
#include <stdint.h>

typedef unsigned v4u __attribute__((ext_vector_type(4)));
typedef unsigned v2u __attribute__((ext_vector_type(2)));
typedef short s4 __attribute__((ext_vector_type(4)));
typedef short s8 __attribute__((ext_vector_type(8)));
typedef __bf16 bf2 __attribute__((ext_vector_type(2)));
typedef float f4v __attribute__((ext_vector_type(4)));
typedef float f2v __attribute__((ext_vector_type(2)));
#define LAS __attribute__((address_space(3)))
#define DI __device__ __forceinline__
#define SB() __builtin_amdgcn_sched_barrier(0)

constexpr unsigned L_KP = 0, L_W = 32768;
constexpr unsigned L_X = 98304;
constexpr unsigned L_V = 131072;
constexpr unsigned L_M = 147456;
constexpr unsigned LDS_BYTES = 163840;
constexpr unsigned t_slot(int t) { return (unsigned)(t & 3) * 32768u; }
constexpr unsigned w_slot(int j) { return (unsigned)(j % 3) * 32768u; }
constexpr size_t WS_N = 0, WS_I = 4096, WS_P = 65536;

template <int N> struct IC { static constexpr int value = N; };
template <int I, int N, class F> DI void static_for(F&& f) { if constexpr (I < N) { f(IC<I>{}); static_for<I + 1, N>(f); } }

struct Piece { unsigned v0, v1, v2, v3; const char* sb; unsigned lds; };
#define GL4(P) "global_load_lds_dwordx4 %0, %4" P "\n\tglobal_load_lds_dwordx4 %1, %4 offset:1024" P "\n\t" \
               "global_load_lds_dwordx4 %2, %4 offset:2048" P "\n\tglobal_load_lds_dwordx4 %3, %4 offset:3072" P
#define DMA_OPS "v"(p.v0), "v"(p.v1), "v"(p.v2), "v"(p.v3), "s"(p.sb), "s"(p.lds)
DI void dma4(const Piece& p) { asm volatile("s_mov_b32 m0, %5\n\ts_nop 0\n\t" GL4("") :: DMA_OPS : "memory"); }
DI void dma4_dev(const Piece& p) { asm volatile("s_mov_b32 m0, %5\n\ts_nop 0\n\t" GL4(" sc1") :: DMA_OPS : "memory"); }
template <int N> DI void wb_dma4(const Piece& p) { asm volatile("s_mov_b32 m0, %5\n\ts_waitcnt vmcnt(%6)\n\ts_barrier\n\t" GL4("") :: DMA_OPS, "i"(N) : "memory"); }
template <int N> DI void wb_dma4_dev(const Piece& p) { asm volatile("s_mov_b32 m0, %5\n\ts_waitcnt vmcnt(%6)\n\ts_barrier\n\t" GL4(" sc1") :: DMA_OPS, "i"(N) : "memory"); }
DI void dma1(const Piece& p) { asm volatile("s_mov_b32 m0, %2\n\ts_nop 0\n\tglobal_load_lds_dwordx4 %0, %1" :: "v"(p.v0), "s"(p.sb), "s"(p.lds) : "memory"); }
template <int N> DI void wb_dma3(const Piece& p) {
  asm volatile("s_mov_b32 m0, %4\n\ts_waitcnt vmcnt(%5)\n\ts_barrier\n\tglobal_load_lds_dwordx4 %0, %3 offset:1024\n\t"
               "global_load_lds_dwordx4 %1, %3 offset:2048\n\tglobal_load_lds_dwordx4 %2, %3 offset:3072"
               :: "v"(p.v1), "v"(p.v2), "v"(p.v3), "s"(p.sb), "s"(p.lds), "i"(N) : "memory");
}
DI void touch(const char* p, unsigned lds) { asm volatile("s_mov_b32 m0, %1\n\ts_nop 0\n\tglobal_load_lds_dword %0, off" :: "v"(p), "s"(lds) : "memory"); }
template <int N> DI void waitv() { asm volatile("s_waitcnt vmcnt(%0)" :: "i"(N) : "memory"); }
DI void bar() { asm volatile("s_barrier" ::: "memory"); }
DI void barl() { asm volatile("s_waitcnt lgkmcnt(0)\n\ts_barrier" ::: "memory"); }
DI void aload(unsigned& d, unsigned off, const char* base) { asm volatile("global_load_dword %0, %1, %2 sc1" : "=v"(d) : "v"(off), "s"(base) : "memory"); }
DI void astore(unsigned off, const char* base, unsigned v) { asm volatile("global_store_dword %0, %1, %2 sc1" :: "v"(off), "v"(v), "s"(base) : "memory"); }
DI unsigned differs(unsigned a, unsigned b) { unsigned long long m; asm volatile("v_cmp_ne_u32 %0, %1, %2" : "=s"(m) : "v"(a), "v"(b) : "memory"); return (unsigned)m; }
DI void keep(unsigned x) { asm volatile("" :: "v"(x)); }

DI s4 tr(unsigned a) { return __builtin_amdgcn_ds_read_tr16_b64_v4i16((LAS s4*)(size_t)a); }
DI s8 cat(s4 lo, s4 hi) { return __builtin_shufflevector(lo, hi, 0, 1, 2, 3, 4, 5, 6, 7); }
DI s8 rd16(unsigned a) { return *(LAS s8*)(size_t)a; }
DI s4 rd8(unsigned a) { return *(LAS s4*)(size_t)a; }
#define MF "v_mfma_f32_16x16x32_bf16 %0, %1, %2, "
DI void mma(f4v& c, s8 a, s8 b) { asm volatile(MF "%0" : "+v"(c) : "v"(a), "v"(b)); }
DI void mma(f4v& c, s8 a, s8 b, s8 e0) { asm volatile(MF "%0" : "+v"(c) : "v"(a), "v"(b), "v"(e0)); }
DI void mma(f4v& c, s8 a, s8 b, s8 e0, s8 e1) { asm volatile(MF "%0" : "+v"(c) : "v"(a), "v"(b), "v"(e0), "v"(e1)); }
DI void mma(f4v& c, s8 a, s8 b, s8 e0, s8 e1, s8 e2) { asm volatile(MF "%0" : "+v"(c) : "v"(a), "v"(b), "v"(e0), "v"(e1), "v"(e2)); }
DI void mma0(f4v& c, s8 a, s8 b) { asm volatile(MF "0" : "=&v"(c) : "v"(a), "v"(b)); }
DI void mma0(f4v& c, s8 a, s8 b, s8 e0) { asm volatile(MF "0" : "=&v"(c) : "v"(a), "v"(b), "v"(e0)); }
DI void mfma_done(f4v& a, f4v& b, f4v& c, f4v& d) { asm volatile("s_nop 7\n\ts_nop 1" : "+v"(a), "+v"(b), "+v"(c), "+v"(d)); }
DI unsigned pk(float a, float b) { f2v v = {a, b}; return __builtin_bit_cast(unsigned, __builtin_convertvector(v, bf2)); }
DI unsigned opaque(unsigned x) { asm volatile("" : "+v"(x)); return x; }
DI unsigned vmax(unsigned a, unsigned b) { unsigned r; asm("v_max_f32 %0, %1, %2" : "=v"(r) : "v"(a), "v"(b)); return r; }

struct Args {
  const char* in[2][5];
  char* out; char* ws;
};

__global__ __launch_bounds__(512, 2) void gqa_fused_kernel(Args a) {
#if __HIP_DEVICE_COMPILE__
  const int lane = threadIdx.x & 63;
  const int wave = __builtin_amdgcn_readfirstlane(threadIdx.x >> 6);
  const int xcd = blockIdx.x & 7, idx = blockIdx.x >> 3;
  const int br = xcd >> 2, hg = xcd & 3, i8 = idx & 7, rb = idx >> 3;
  const int head = hg * 8 + i8;
  const int ct = hg * 4 + (i8 & 3), rp = rb * 2 + (i8 >> 2);

  if (__builtin_expect(wave < 4, 1)) {
    const int lw = wave;
    const char* x = a.in[br][0]; const char* kc = a.in[br][1]; const char* vc = a.in[br][2]; const char* wq = a.in[br][3]; const char* wo = a.in[br][4];
    char* ws = a.ws;
    unsigned vo[8], vk[8], vw[4];
    if (lw < 2) {
#pragma unroll
      for (int i = 0; i < 8; ++i) { const int row = lw * 64 + i * 8 + (lane >> 3); vo[i] = row * 2048 + (((lane & 7) ^ ((row >> 1) & 7)) << 4) + 4096 - (i & 3) * 1024; }
    } else {
#pragma unroll
      for (int i = 0; i < 8; ++i) { const int k = (lw - 2) * 32 + i * 4 + (lane >> 4); const int f = ((k & 3) | (((k >> 3) & 1) << 2)) << 1;
        vo[i] = k * 8192 + (((lane & 15) ^ f) << 4) + 4096 - (i & 3) * 1024; }
    }
    asm volatile("" : "+s"(x), "+s"(kc), "+s"(vc), "+s"(wq), "+s"(wo), "+s"(ws));
    int rot1 = rb + (i8 & 1) + (i8 >> 2) * 2;
    const bool shift1 = (i8 & 2) != 0;
    auto ktile = [&](int s) {
      const int plain = (s & 12) + ((s + rot1) & 3);
      const int shifted = (s < 2 || s >= 14) ? (s & 14) + ((s + rot1) & 1) : 2 + ((s - 2) & 12) + ((s - 2 + rot1) & 3);
      return ((shift1 ? shifted : plain) + 4 * hg) & 15; };
    const char* const sb = lw < 2 ? x + (size_t)rb * 128 * 2048 - 4096 : wq + head * 256 - 4096;
    const unsigned tstride = lw < 2 ? 128 : 64 * 8192;
    auto pT = [&](int t, int half) { return Piece{vo[4 * half], vo[4 * half + 1], vo[4 * half + 2], vo[4 * half + 3], sb + ktile(t) * tstride,
                                                  t_slot(t) + lw * 8192 + half * 4096}; };
    dma4(pT(0, 0)); dma4(pT(0, 1));
    asm volatile("" : "+s"(rot1));
    dma4(pT(1, 0)); dma4(pT(1, 1)); dma1(pT(2, 0));
#pragma unroll
    for (int i = 0; i < 8; ++i) { const int row = i * 4 + (lane >> 4); vk[i] = row * 256 + (((lane & 15) ^ (row & 15)) << 4) + 4096 - (i & 3) * 1024; }
#pragma unroll
    for (int i = 0; i < 4; ++i) { const int f = ((lane >> 4) & 1) | ((i & 1) << 1);
      vw[i] = (lw * 32 + i * 8 + (lane >> 3)) * 2048 + (((lane & 7) ^ (f << 1)) << 4) + 4096 - i * 1024; }
    const unsigned vv0 = (lw & 1) ? vk[4] : vk[0], vv1 = (lw & 1) ? vk[5] : vk[1], vv2 = (lw & 1) ? vk[6] : vk[2], vv3 = (lw & 1) ? vk[7] : vk[3];
    const unsigned vp = lane * 16, vl4 = (lane & 31) * 4;
    const int g0 = (ct + 2 * rp) & 3, rot2 = (rp >> 1) & 3;
    auto grp = [&](int k) { return (g0 + ((ct & 2) ? ((k >> 1) | ((k & 1) << 1)) : k)) & 3; };
    auto hp = [&](int j) { return grp(j >> 2) * 4 + ((j + rot2) & 3); };
    const char* const kb = kc + (size_t)((rb * 4 + lw) * 4 + hg) * 8192;
    auto pK = [&](int half) { return Piece{vk[4 * half], vk[4 * half + 1], vk[4 * half + 2], vk[4 * half + 3], kb - 4096, L_KP + lw * 8192 + half * 4096}; };
    auto pW = [&](int j, int half) { return Piece{vw[0], vw[1], vw[2], vw[3], wo + (size_t)(hp(j) * 2 + half) * 128 * 2048 + ct * 128 - 4096,
                                                  L_W + w_slot(j) + half * 16384 + lw * 4096}; };
    auto pV = [&](int k) { return Piece{vv0, vv1, vv2, vv3, vc + (size_t)((rp * 2 + (lw >> 1)) * 4 + grp(k)) * 8192 - 4096, L_V + lw * 4096}; };
    auto pP = [&](int m) { return Piece{vp, vp, vp, vp, ws + WS_P + (size_t)((br * 16 + rp * 2 + (lw >> 1)) * 32 + hp(2 * m + (lw & 1)) * 2) * 2048,
                                        L_KP + (m & 1) * 16384 + lw * 4096}; };
    const char* const gN = ws + WS_N + (br * 4 + rb) * 128;
    const char* const gI = ws + WS_I + (br * 4 + rb) * 4096;
    const char* const myI = gI + head * 128;
    unsigned entry, nonce, nused, nfin, poll[3];
    const int slowc = (3 - (int)((size_t)wo >> 7)) & 7, tp = (rb * 2 + (i8 >> 2)) * 4 + lw;
    const int tstep = (slowc >> 2) == (hg & 1) && (i8 & 3) == 0 ? (lw < 2 ? 6 : 10) : -1;
    const char* const tptr = wo + (size_t)(((((slowc + 1 + 2 * (tp >> 4)) & 3) * 4 + ((tp >> 2) & 3)) * 256 + (tp & 3) * 64 + lane) * 2048) + ((hg & 2) * 4 + slowc) * 128;
    static_for<0, 14>([&](auto nn) {
      constexpr int n = nn.value;
      if constexpr (n >= 9 && n <= 12) { if (shift1 == (n <= 10)) wb_dma3<14>(pT(n + 2, 0)); else wb_dma3<9>(pT(n + 2, 0)); }
      else wb_dma3<(n == 1 ? 10 : 9)>(pT(n + 2, 0));
      dma4(pT(n + 2, 1));
      if constexpr (n == 8 || n == 10) { if (shift1 == (n == 8)) { touch(kb + (i8 * 8 + (lane & 7)) * 128, L_M + 2048 + lw * 256); dma4(pV(0)); } }
      if constexpr (n == 0) aload(entry, vl4, myI);
      if constexpr (n == 2) {
        entry = opaque(entry);
        if (lw == 0) {
          unsigned long long live = 0xffffffffull; unsigned nv = 0;
#pragma unroll
          for (int b = 0; b < 6; ++b) {
            const unsigned long long one = __builtin_amdgcn_ballot_w64((entry >> b) & 1u) & live, zero = live & ~one;
            const bool pick1 = __builtin_popcountll(one) < __builtin_popcountll(zero);
            nv |= pick1 ? (1u << b) : 0u; live = pick1 ? one : zero;
          }
          astore(head * 4, gN, nv);
          *(volatile LAS unsigned*)(size_t)(L_M + 16) = nv;
          asm volatile("s_waitcnt lgkmcnt(0)" ::: "memory");
        }
      }
      if constexpr (n == 4) nonce = opaque(*(volatile LAS unsigned*)(size_t)(L_M + 16));
      if constexpr (n == 6 || n == 10) { if (n == tstep) touch(tptr, L_M + 1024 + lw * 256); }
      if constexpr (n < 13) dma1(pT(n + 3, 0)); else dma1(pK(0));
    });
    wb_dma3<9>(pK(0)); dma4(pK(1)); dma1(pW(0, 0));
    wb_dma3<9>(pW(0, 0)); dma4(pW(0, 1)); aload(nused, vl4, gN); dma1(pW(1, 0));
    wb_dma3<10>(pW(1, 0)); dma4(pW(1, 1));
    wb_dma4<8>(pW(2, 0)); dma4(pW(2, 1));
    static_for<1, 16>([&](auto jj) {
      constexpr int j = jj.value, m = j - 7;
      constexpr bool hasP = m >= 0 && m < 8, hasV = j % 4 == 1 && j <= 9, hasW = j + 2 <= 15;
      constexpr int N = j == 15 ? 0 : j == 10 ? 9 : j == 14 ? 8 : 8;
      auto rest = [&]() { if constexpr (hasV) dma4(pV(j / 4 + 1)); if constexpr (hasW) { dma4(pW(j + 2, 0)); dma4(pW(j + 2, 1)); } };
      if constexpr (j == 1) {
        waitv<N>(); bar();
        if (lw == 0) astore(vl4 * 32, gI + head * 4, nused);
        rest();
      } else if constexpr (j == 6) { waitv<N>(); bar(); aload(poll[0], vl4, myI); rest(); }
      else if constexpr (j == 7 || j == 8) {
        const Piece pp = pP(m);
        const unsigned need = j == 8 ? ~0u : 3u << (2 * hp(lw & 1));
        waitv<N>(); bar(); aload(poll[m + 1], vl4, myI);
        if (__builtin_expect((differs(poll[m], nonce) & need) == 0, 1)) { dma4_dev(pp); rest(); }
        else {
          rest(); waitv<8>();
          if ((differs(poll[m + 1], nonce) & need) != 0) {
            _Pragma("unroll 1") for (unsigned spins = 0; spins < (1u << 20); ++spins) {
              unsigned f; aload(f, vl4, myI); waitv<0>();
              if ((differs(f, nonce) & need) == 0) break;
              __builtin_amdgcn_s_sleep(1);
            }
          }
          dma4_dev(pp); waitv<0>();
        }
      }
      else if constexpr (hasP) { wb_dma4_dev<N>(pP(m)); rest(); }
      else if constexpr (hasV) { wb_dma4<N>(pV(j / 4 + 1)); dma4(pW(j + 2, 0)); dma4(pW(j + 2, 1)); }
      else if constexpr (hasW) { wb_dma4<N>(pW(j + 2, 0)); dma4(pW(j + 2, 1)); }
      else { waitv<N>(); bar(); }
      if constexpr (j == 9) { keep(poll[2]); aload(nfin, vl4, gN); }
      if constexpr (j == 11) { if (lw == 0 && differs(nfin, nused) != 0) astore(vl4 * 32, gI + head * 4, nfin); }
    });
  } else {
    __builtin_amdgcn_s_setprio(1);
    const int w = wave - 4;
    const int g = lane >> 4, q4 = (lane & 15) >> 2, p = lane & 3, r = lane & 15;
    const int rh = w >> 1, dh = w & 1;
    char* pbase = a.ws + WS_P + (size_t)((br * 16 + rb * 4 + w) * 32 + head) * 2048;
    char* obase = a.out + (size_t)((rp * 2 + rh) * 32) * 4096 + br * 2048 + ct * 128 + dh * 64;
    asm volatile("" : "+s"(pbase), "+s"(obase));
    const unsigned ooff = opaque(r * 4096 + ((g & 1) * 16 + (g >> 1) * 8) * 2);
    unsigned aW[2][2], aV[4], aA[2][4], aB[2][2][2], aK[2][4];
#pragma unroll
    for (int nb = 0; nb < 2; ++nb) { aW[0][nb] = opaque(L_W + (8 * g + q4) * 128 + (((dh * 2 + nb) ^ ((q4 >> 1) | ((g & 1) << 1))) << 5) + p * 8); aW[1][nb] = opaque(aW[0][nb] + 65536); }
    const unsigned aP = opaque(L_KP + rh * 8192 + lane * 16);
#pragma unroll
    for (int s = 0; s < 4; ++s) aV[s] = opaque(L_V + (rh * 32 + r) * 256 + (((4 * s + g) ^ r) << 4));
#pragma unroll
    for (int c = 0; c < 4; ++c) { aA[0][c] = opaque(16384 + (8 * g + q4) * 256 + (((dh * 4 + c) ^ (q4 | ((g & 1) << 2))) << 5) + p * 8); aA[1][c] = opaque(aA[0][c] + 65536); }
#pragma unroll
    for (int kk = 0; kk < 2; ++kk)
#pragma unroll
      for (int o = 0; o < 2; ++o) { const int row = rh * 64 + ((o ^ dh ^ 1) << 5) + r;
        aB[0][kk][o] = opaque(row * 128 + (((4 * kk + g) ^ ((row >> 1) & 7)) << 4)); aB[1][kk][o] = opaque(aB[0][kk][o] + 65536); }
#pragma unroll
    for (int o = 0; o < 2; ++o)
#pragma unroll
      for (int i = 0; i < 4; ++i) aK[o][i] = opaque(L_KP + ((rh * 2 + (o ^ dh ^ 1)) * 32 + r) * 256 + (g & 1) * 8 + (((dh * 8 + 2 * i + (g >> 1)) ^ r) << 4));
    const unsigned aXw = opaque(L_X + w * 4096 + lane * 16), aXr = opaque(L_X + (w ^ 1) * 4096 + lane * 16);
    const unsigned aFw = opaque(L_M + w * 4), aFr = opaque(L_M + (w ^ 1) * 4);

    f4v acc[4][4];
    s4 Al[4][2], Ah[4][2]; s8 B[2][4][2];
    auto rdA = [&](auto tile, auto cc, auto ii) { constexpr int c = cc.value, kk = ii.value >> 1;
      constexpr unsigned so = t_slot(tile.value); constexpr int hi = so >= 65536; constexpr unsigned o = so - hi * 65536 + kk * 8192;
      if constexpr (ii.value & 1) Ah[c][kk] = tr(aA[hi][c] + o + 1024); else Al[c][kk] = tr(aA[hi][c] + o); };
    auto rdB = [&](auto tile, auto bb, auto kk) { constexpr int b = bb.value;
      constexpr unsigned so = t_slot(tile.value); constexpr int hi = so >= 65536; constexpr unsigned o = so - hi * 65536;
      B[tile.value & 1][b][kk.value] = rd16(aB[hi][kk.value][b >> 1] + o + (b & 1) * 2048); };
    auto rdNext = [&](auto tile, auto ii) { constexpr int i = ii.value;
      if constexpr (i < 2) rdB(tile, IC<0>{}, ii); else if constexpr (i < 6) rdA(tile, IC<0>{}, IC<i - 2>{});
      else rdB(tile, IC<(i - 4) / 2>{}, IC<i & 1>{}); };
    auto mm1 = [&](auto tile, auto cc, auto bb, auto kc) { constexpr int t = tile.value, c = cc.value, b = bb.value, kk = kc.value;
      const s8 av = cat(Al[c][kk], Ah[c][kk]), bv = B[t & 1][b][kk];
      SB();
      if constexpr (t == 0 && kk == 0) mma0(acc[c][b], av, bv);
      else if constexpr (kk == 0 && c == 0 && b == 0) mma(acc[c][b], av, bv, cat(Al[0][1], Ah[0][1]), B[t & 1][1][1]);
      else if constexpr (kk == 0 && c == 0 && b == 2) mma(acc[c][b], av, bv, B[t & 1][3][1]);
      else if constexpr (kk == 0 && c == 1 && b == 0) mma(acc[c][b], av, bv, cat(Al[1][1], Ah[1][1]));
      else if constexpr (kk == 0 && c == 2 && b == 0) mma(acc[c][b], av, bv, cat(Al[2][1], Ah[2][1]), cat(Al[3][0], Ah[3][0]), cat(Al[3][1], Ah[3][1]));
      else mma(acc[c][b], av, bv);
      SB(); };
    *(volatile LAS unsigned*)(size_t)aFw = 0u;
    barl();
    static_for<0, 12>([&](auto ii) { rdNext(IC<0>{}, ii); });
    static_for<0, 16>([&](auto t) {
      static_for<0, 16>([&](auto mm) { constexpr int m = mm.value;
        mm1(t, IC<(m >> 3)>{}, IC<((m >> 1) & 3)>{}, IC<(m & 1)>{});
        if constexpr (m < 12) rdA(t, IC<1 + (m >> 2)>{}, IC<(m & 3)>{}); });
      barl();
      if constexpr (t.value < 15) static_for<0, 16>([&](auto mm) { constexpr int m = mm.value;
        mm1(t, IC<2 + (m >> 3)>{}, IC<((m >> 1) & 3)>{}, IC<(m & 1)>{});
        if constexpr (m < 12) rdNext(IC<t.value + 1>{}, mm); });
    });
    s4 Kh[2][2][2][2];
    unsigned q[4][2][4];
    f4v St[2][2][2];
    auto rdK = [&](auto ii) { constexpr int i = ii.value, o = i >> 3, kb = (i >> 2) & 1, s = (i >> 1) & 1, u = i & 1;
      Kh[o][kb][s][u] = rd8(aK[o][s * 2 + u] + kb * 4096); };
    auto cvq = [&](auto bb, auto cc) { constexpr int b = bb.value, c = cc.value;
      q[b][c >> 1][(c & 1) * 2] = opaque(pk(acc[c][b][0], acc[c][b][1])); q[b][c >> 1][(c & 1) * 2 + 1] = opaque(pk(acc[c][b][2], acc[c][b][3])); };
    static_for<0, 16>([&](auto mm) { constexpr int m = mm.value;
      constexpr int cb[16] = {0, 0, 1, 1, 0, 0, 2, 2, 3, 3, 1, 1, 9, 2, 2, 9}, cc[16] = {0, 1, 0, 1, 2, 3, 0, 1, 0, 1, 2, 3, 0, 2, 3, 0};
      mm1(IC<15>{}, IC<2 + ((m >> 1) & 1)>{}, IC<(m >> 2)>{}, IC<(m & 1)>{});
      rdK(mm);
      if constexpr (cb[m] < 4) cvq(IC<cb[m] & 3>{}, IC<cc[m]>{}); });
    static_for<0, 16>([&](auto mm) { constexpr int m = mm.value, o = m >> 3, qb = (m >> 2) & 1, kb = (m >> 1) & 1, s = m & 1;
      const v4u qq = {q[2 * o + qb][s][0], q[2 * o + qb][s][1], q[2 * o + qb][s][2], q[2 * o + qb][s][3]};
      SB();
      if constexpr (s == 0) mma0(St[o][kb][qb], cat(Kh[o][kb][s][0], Kh[o][kb][s][1]), __builtin_bit_cast(s8, qq));
      else mma(St[o][kb][qb], cat(Kh[o][kb][s][0], Kh[o][kb][s][1]), __builtin_bit_cast(s8, qq));
      SB();
      if constexpr (m == 4 || m == 5) cvq(IC<3>{}, IC<(m - 2) & 3>{});
      if constexpr (m == 9 || m == 10 || m == 12 || m == 14) { constexpr int t = m == 9 ? 0 : m == 10 ? 1 : m == 12 ? 2 : 3;
        *(LAS f4v*)(size_t)(aXw + t * 1024) = St[0][t & 1][t >> 1]; } });
    SB();
    asm volatile("s_waitcnt lgkmcnt(0)" ::: "memory");
    *(volatile LAS unsigned*)(size_t)aFw = 1u;
    while (__builtin_amdgcn_readfirstlane(*(volatile LAS unsigned*)(size_t)aFr) == 0u) {}
    asm volatile("s_nop 3" : "+v"(St[1][0][0]), "+v"(St[1][0][1]), "+v"(St[1][1][0]), "+v"(St[1][1][1]) :: "memory");
#pragma unroll
    for (int t = 0; t < 4; ++t) St[1][t & 1][t >> 1] += *(LAS f4v*)(size_t)(aXr + t * 1024);
    const float cs = 0.08838834764831845f * 1.4426950408889634f;
    f4v e[2][2];
    float m2[2], s2[2];
#pragma unroll
    for (int qb = 0; qb < 2; ++qb) { const f4v u = St[1][0][qb], v = St[1][1][qb];
      m2[qb] = fmaxf(fmaxf(fmaxf(fmaxf(u[0], u[1]), u[2]), fmaxf(fmaxf(u[3], v[0]), v[1])), fmaxf(v[2], v[3])); }
    v2u t0 = __builtin_amdgcn_permlane16_swap(__float_as_uint(m2[0]), __float_as_uint(m2[1]), false, false);
    unsigned z = vmax(t0[0], t0[1]);
    t0 = __builtin_amdgcn_permlane32_swap(z, z, false, false);
    z = vmax(t0[0], t0[1]);
    t0 = __builtin_amdgcn_permlane16_swap(z, z, false, false);
#pragma unroll
    for (int qb = 0; qb < 2; ++qb) {
#pragma unroll
      for (int kb = 0; kb < 2; ++kb) {
        const f4v y = (St[1][kb][qb] - __uint_as_float(t0[qb])) * cs;
#pragma unroll
        for (int i = 0; i < 4; ++i) e[qb][kb][i] = __builtin_amdgcn_exp2f(y[i]);
      }
      const f4v sm = e[qb][0] + e[qb][1];
      s2[qb] = (sm[0] + sm[2]) + (sm[1] + sm[3]);
    }
    t0 = __builtin_amdgcn_permlane16_swap(__float_as_uint(s2[0]), __float_as_uint(s2[1]), false, false);
    z = __float_as_uint(__uint_as_float(t0[0]) + __uint_as_float(t0[1]));
    t0 = __builtin_amdgcn_permlane32_swap(z, z, false, false);
    z = __float_as_uint(__builtin_amdgcn_rcpf(__uint_as_float(t0[0]) + __uint_as_float(t0[1])));
    t0 = __builtin_amdgcn_permlane16_swap(z, z, false, false);
    const __amdgpu_buffer_rsrc_t prs = __builtin_amdgcn_make_buffer_rsrc(pbase, 0, 2048, 0x00020000);
#pragma unroll
    for (int qb = 0; qb < 2; ++qb) {
      const f4v u = e[qb][0] * __uint_as_float(t0[qb]), v = e[qb][1] * __uint_as_float(t0[qb]);
      const v4u pf = {pk(u[0], u[1]), pk(u[2], u[3]), pk(v[0], v[1]), pk(v[2], v[3])};
      __builtin_amdgcn_raw_buffer_store_b128(pf, prs, lane * 16 + qb * 1024, 0, 16);
    }

    s8 Vf[2][4];
    s4 Wl[4][4], Wh[4][4];
    f4v X[2][2];
    unsigned Xb[32][2][4];
    s8 Pf[4][2];
    f4v out[2][2];
    auto rdW = [&](auto rr) { constexpr int rho = rr.value, u = rho >> 3, s = (rho >> 1) & 3;
      constexpr unsigned so = w_slot(u >> 2); constexpr int hi = so >= 65536;
      constexpr unsigned o = so - hi * 65536 + ((u >> 1) & 1) * 16384 + s * 4096 + (rho & 1) * 512;
      if constexpr (rho & 1) Wh[u & 3][s] = tr(aW[hi][u & 1] + o); else Wl[u & 3][s] = tr(aW[hi][u & 1] + o); };
    auto rdV = [&](auto kb, auto s) { Vf[kb.value][s.value] = rd16(aV[s.value] + kb.value * 4096); };
    auto rdP = [&](auto pp, auto ii) { constexpr int h = 2 * pp.value + (ii.value & 1), qb = ii.value >> 1;
      Pf[h & 3][qb] = rd16(aP + ((h >> 2) & 1) * 16384 + (h & 3) * 2048 + qb * 1024); };
    auto cvt = [&](auto uu, auto ii) { constexpr int u = uu.value, i = ii.value;
      Xb[u >> 1][u & 1][i] = opaque(pk(X[u & 1][i >> 1][(i & 1) * 2], X[u & 1][i >> 1][(i & 1) * 2 + 1])); };
    auto pvm = [&](auto pp, auto nn, auto qq, auto ee) { constexpr int h = 2 * pp.value + ee.value, nb = nn.value, qb = qq.value;
      const v4u xa = {Xb[h][nb][0], Xb[h][nb][1], Xb[h][nb][2], Xb[h][nb][3]};
      SB();
      if constexpr (h == 0) mma0(out[nb][qb], __builtin_bit_cast(s8, xa), Pf[h & 3][qb]);
      else if constexpr (nb == 0 && ee.value == 0) mma(out[nb][qb], __builtin_bit_cast(s8, xa), Pf[h & 3][qb], Pf[(h + 1) & 3][qb]);
      else mma(out[nb][qb], __builtin_bit_cast(s8, xa), Pf[h & 3][qb]);
      SB(); };
    barl();
    static_for<0, 4>([&](auto s) { rdW(IC<2 * s.value>{}); rdW(IC<2 * s.value + 1>{}); rdV(IC<0>{}, s); });
    static_for<0, 4>([&](auto s) { rdV(IC<1>{}, s); });
    rdW(IC<8>{}); rdW(IC<9>{});
    static_for<0, 64>([&](auto uu) {
      constexpr int u = uu.value;
      static_for<0, 8>([&](auto ii) {
        constexpr int i = ii.value, kb = i >> 2, s = i & 3, rho = 8 * u + i + 10;
        const s8 wv = cat(Wl[u & 3][s], Wh[u & 3][s]);
        SB();
        if constexpr (i == 0) mma0(X[u & 1][kb], Vf[kb][s], wv, cat(Wl[u & 3][1], Wh[u & 3][1]));
        else if constexpr (i == 4) mma0(X[u & 1][kb], Vf[kb][s], wv);
        else if constexpr (i == 2) mma(X[u & 1][kb], Vf[kb][s], wv, cat(Wl[u & 3][3], Wh[u & 3][3]));
        else mma(X[u & 1][kb], Vf[kb][s], wv);
        SB();
        if constexpr ((u & 3) == 3 && u < 32) {
          if constexpr (i == 1) { if constexpr (u == 3) waitv<0>();
                                  barl(); }
          constexpr int lo[8] = {0, 0, 2, 4, 5, 6, 8, 9}, n[8] = {0, 2, 2, 1, 1, 2, 1, 1};
          static_for<0, n[i]>([&](auto k) { rdW(IC<8 * (u + 1) + lo[i] + k.value>{}); });
        } else if constexpr (rho < 512 && !((u & 3) == 2 && i >= 6)) rdW(IC<rho>{});
        if constexpr (i == 7) cvt(uu, IC<0>{});
        if constexpr (u >= 1 && (i == 0 || i == 3 || i == 4)) cvt(IC<(u >= 1 ? u - 1 : 0)>{}, IC<(i == 0 ? 1 : (i - 1) & 3)>{});
        if constexpr (u == 32 && ((i & 3) == 1 || (i & 3) == 2)) rdP(IC<0>{}, IC<((i >> 2) * 2 + (i & 3) - 1) & 3>{});
        if constexpr ((u & 15) == 15 && u < 63 && i >= 2) rdV(IC<((i - 2) >> 2) & 1>{}, IC<(i - 2) & 3>{});
        if constexpr ((u & 15) == 0 && u > 0 && i < 2) rdV(IC<1>{}, IC<(i + 2) & 3>{});
      });
      if constexpr (u >= 32 && u <= 60 && (u & 1) == 0)
        static_for<0, 8>([&](auto ii) { constexpr int i = ii.value, pp = ((u - 32) / 2) & 15;
          pvm(IC<pp>{}, IC<((i >> 1) & 1)>{}, IC<(i >> 2)>{}, IC<(i & 1)>{});
          if constexpr ((u & 3) == 0 && i >= 4) rdP(IC<pp + 1>{}, IC<(i & 3)>{});
          if constexpr ((u & 3) == 2) {
            if constexpr (i == 1) barl();
            if constexpr (i >= 2 && i < 6) rdP(IC<pp + 1>{}, IC<(i - 2) & 3>{});
            if constexpr (i >= 6) rdW(IC<8 * (u + 2) + i - 6>{});
          } });
    });
    static_for<0, 4>([&](auto ii) { pvm(IC<15>{}, IC<0>{}, IC<(ii.value >> 1)>{}, IC<(ii.value & 1)>{});
      if constexpr (ii.value == 0) cvt(IC<63>{}, IC<1>{}); });
    asm volatile("s_nop 3"); SB();
    cvt(IC<63>{}, IC<2>{}); cvt(IC<63>{}, IC<3>{});
    SB(); asm volatile("s_nop 1");
    static_for<0, 4>([&](auto ii) { pvm(IC<15>{}, IC<1>{}, IC<(ii.value >> 1)>{}, IC<(ii.value & 1)>{}); });
    mfma_done(out[0][0], out[0][1], out[1][0], out[1][1]);
    const __amdgpu_buffer_rsrc_t ors = __builtin_amdgcn_make_buffer_rsrc(obase, 0, 31 * 4096 + 64, 0x00020000);
#pragma unroll
    for (int qb = 0; qb < 2; ++qb) {
      const v2u s0 = __builtin_amdgcn_permlane16_swap(pk(out[0][qb][0], out[0][qb][1]), pk(out[1][qb][0], out[1][qb][1]), false, false);
      const v2u s1 = __builtin_amdgcn_permlane16_swap(pk(out[0][qb][2], out[0][qb][3]), pk(out[1][qb][2], out[1][qb][3]), false, false);
      const v4u o4 = {s0[0], s1[0], s0[1], s1[1]};
      __builtin_amdgcn_raw_buffer_store_b128(o4, ors, ooff + qb * 16 * 4096, 0, 18);
    }
    __builtin_amdgcn_endpgm();
  }
#endif
}

extern "C" void kernel_launch(void* const* d_in, const int* in_sizes, int n_in, void* d_out, int out_size, void* d_ws, size_t ws_size,
                              hipStream_t stream) {
  Args a;
  for (int i = 0; i < 10; ++i) a.in[i / 5][i % 5] = (const char*)d_in[i];
  a.out = (char*)d_out; a.ws = (char*)d_ws;
  (void)hipFuncSetAttribute((const void*)gqa_fused_kernel, hipFuncAttributeMaxDynamicSharedMemorySize, LDS_BYTES);
  gqa_fused_kernel<<<256, 512, LDS_BYTES, stream>>>(a);
}
```

```cpp
#include <hip/hip_runtime.h>
#include <hip/hip_bf16.h>
#include <stdint.h>

typedef unsigned v4u __attribute__((ext_vector_type(4)));
typedef unsigned v2u __attribute__((ext_vector_type(2)));
typedef short s4 __attribute__((ext_vector_type(4)));
typedef short s8 __attribute__((ext_vector_type(8)));
typedef __bf16 bf2 __attribute__((ext_vector_type(2)));
typedef float f4v __attribute__((ext_vector_type(4)));
typedef float f2v __attribute__((ext_vector_type(2)));
#define LAS __attribute__((address_space(3)))
#define DI __device__ __forceinline__
#define SB() __builtin_amdgcn_sched_barrier(0)

constexpr unsigned L_KP = 0, L_W = 32768;
constexpr unsigned L_X = 98304;
constexpr unsigned L_V = 131072;
constexpr unsigned L_M = 147456;
constexpr unsigned LDS_BYTES = 163840;
constexpr unsigned t_slot(int t) { return (unsigned)(t & 3) * 32768u; }
constexpr unsigned w_slot(int j) { return (unsigned)(j % 3) * 32768u; }
constexpr size_t WS_N = 0, WS_I = 4096, WS_P = 65536;

template <int N> struct IC { static constexpr int value = N; };
template <int I, int N, class F> DI void static_for(F&& f) { if constexpr (I < N) { f(IC<I>{}); static_for<I + 1, N>(f); } }

struct Piece { unsigned v0, v1, v2, v3; const char* sb; unsigned lds; };
#define GL4(P) "global_load_lds_dwordx4 %0, %4" P "\n\tglobal_load_lds_dwordx4 %1, %4 offset:1024" P "\n\t" \
               "global_load_lds_dwordx4 %2, %4 offset:2048" P "\n\tglobal_load_lds_dwordx4 %3, %4 offset:3072" P
#define DMA_OPS "v"(p.v0), "v"(p.v1), "v"(p.v2), "v"(p.v3), "s"(p.sb), "s"(p.lds)
DI void dma4(const Piece& p) { asm volatile("s_mov_b32 m0, %5\n\ts_nop 0\n\t" GL4("") :: DMA_OPS : "memory"); }
DI void dma4_dev(const Piece& p) { asm volatile("s_mov_b32 m0, %5\n\ts_nop 0\n\t" GL4(" sc1") :: DMA_OPS : "memory"); }
template <int N> DI void wb_dma4(const Piece& p) { asm volatile("s_mov_b32 m0, %5\n\ts_waitcnt vmcnt(%6)\n\ts_barrier\n\t" GL4("") :: DMA_OPS, "i"(N) : "memory"); }
template <int N> DI void wb_dma4_dev(const Piece& p) { asm volatile("s_mov_b32 m0, %5\n\ts_waitcnt vmcnt(%6)\n\ts_barrier\n\t" GL4(" sc1") :: DMA_OPS, "i"(N) : "memory"); }
DI void dma1(const Piece& p) { asm volatile("s_mov_b32 m0, %2\n\ts_nop 0\n\tglobal_load_lds_dwordx4 %0, %1" :: "v"(p.v0), "s"(p.sb), "s"(p.lds) : "memory"); }
template <int N> DI void wb_dma3(const Piece& p) {
  asm volatile("s_mov_b32 m0, %4\n\ts_waitcnt vmcnt(%5)\n\ts_barrier\n\tglobal_load_lds_dwordx4 %0, %3 offset:1024\n\t"
               "global_load_lds_dwordx4 %1, %3 offset:2048\n\tglobal_load_lds_dwordx4 %2, %3 offset:3072"
               :: "v"(p.v1), "v"(p.v2), "v"(p.v3), "s"(p.sb), "s"(p.lds), "i"(N) : "memory");
}
DI void touch(const char* p, unsigned lds) { asm volatile("s_mov_b32 m0, %1\n\ts_nop 0\n\tglobal_load_lds_dword %0, off" :: "v"(p), "s"(lds) : "memory"); }
template <int N> DI void waitv() { asm volatile("s_waitcnt vmcnt(%0)" :: "i"(N) : "memory"); }
DI void bar() { asm volatile("s_barrier" ::: "memory"); }
DI void barl() { asm volatile("s_waitcnt lgkmcnt(0)\n\ts_barrier" ::: "memory"); }
DI void aload(unsigned& d, unsigned off, const char* base) { asm volatile("global_load_dword %0, %1, %2 sc1" : "=v"(d) : "v"(off), "s"(base) : "memory"); }
DI void astore(unsigned off, const char* base, unsigned v) { asm volatile("global_store_dword %0, %1, %2 sc1" :: "v"(off), "v"(v), "s"(base) : "memory"); }
DI unsigned differs(unsigned a, unsigned b) { unsigned long long m; asm volatile("v_cmp_ne_u32 %0, %1, %2" : "=s"(m) : "v"(a), "v"(b) : "memory"); return (unsigned)m; }
DI void keep(unsigned x) { asm volatile("" :: "v"(x)); }

DI s4 tr(unsigned a) { return __builtin_amdgcn_ds_read_tr16_b64_v4i16((LAS s4*)(size_t)a); }
DI s8 cat(s4 lo, s4 hi) { return __builtin_shufflevector(lo, hi, 0, 1, 2, 3, 4, 5, 6, 7); }
DI s8 rd16(unsigned a) { return *(LAS s8*)(size_t)a; }
DI s4 rd8(unsigned a) { return *(LAS s4*)(size_t)a; }
#define MF "v_mfma_f32_16x16x32_bf16 %0, %1, %2, "
DI void mma(f4v& c, s8 a, s8 b) { asm volatile(MF "%0" : "+v"(c) : "v"(a), "v"(b)); }
DI void mma(f4v& c, s8 a, s8 b, s8 e0) { asm volatile(MF "%0" : "+v"(c) : "v"(a), "v"(b), "v"(e0)); }
DI void mma(f4v& c, s8 a, s8 b, s8 e0, s8 e1) { asm volatile(MF "%0" : "+v"(c) : "v"(a), "v"(b), "v"(e0), "v"(e1)); }
DI void mma(f4v& c, s8 a, s8 b, s8 e0, s8 e1, s8 e2) { asm volatile(MF "%0" : "+v"(c) : "v"(a), "v"(b), "v"(e0), "v"(e1), "v"(e2)); }
DI void mma0(f4v& c, s8 a, s8 b) { asm volatile(MF "0" : "=&v"(c) : "v"(a), "v"(b)); }
DI void mma0(f4v& c, s8 a, s8 b, s8 e0) { asm volatile(MF "0" : "=&v"(c) : "v"(a), "v"(b), "v"(e0)); }
DI void mfma_done(f4v& a, f4v& b, f4v& c, f4v& d) { asm volatile("s_nop 7\n\ts_nop 1" : "+v"(a), "+v"(b), "+v"(c), "+v"(d)); }
DI unsigned pk(float a, float b) { f2v v = {a, b}; return __builtin_bit_cast(unsigned, __builtin_convertvector(v, bf2)); }
DI unsigned opaque(unsigned x) { asm volatile("" : "+v"(x)); return x; }
DI unsigned vmax(unsigned a, unsigned b) { unsigned r; asm("v_max_f32 %0, %1, %2" : "=v"(r) : "v"(a), "v"(b)); return r; }

struct Args {
  const char* in[2][5];
  char* out; char* ws;
};

__global__ __launch_bounds__(512, 2) void gqa_fused_kernel(Args a) {
#if __HIP_DEVICE_COMPILE__
  const int lane = threadIdx.x & 63;
  const int wave = __builtin_amdgcn_readfirstlane(threadIdx.x >> 6);
  const int xcd = blockIdx.x & 7, idx = blockIdx.x >> 3;
  const int br = xcd >> 2, hg = xcd & 3, i8 = idx & 7, rb = idx >> 3;
  const int head = hg * 8 + i8;
  const int ct = hg * 4 + (i8 & 3), rp = rb * 2 + (i8 >> 2);

  if (__builtin_expect(wave < 4, 1)) {
    const int lw = wave;
    const char* x = a.in[br][0]; const char* kc = a.in[br][1]; const char* vc = a.in[br][2]; const char* wq = a.in[br][3]; const char* wo = a.in[br][4];
    char* ws = a.ws;
    unsigned vo[8], vk[8], vw[4];
    if (lw < 2) {
#pragma unroll
      for (int i = 0; i < 8; ++i) { const int row = lw * 64 + i * 8 + (lane >> 3); vo[i] = row * 2048 + (((lane & 7) ^ ((row >> 1) & 7)) << 4) + 4096 - (i & 3) * 1024; }
    } else {
#pragma unroll
      for (int i = 0; i < 8; ++i) { const int k = (lw - 2) * 32 + i * 4 + (lane >> 4); const int f = ((k & 3) | (((k >> 3) & 1) << 2)) << 1;
        vo[i] = k * 8192 + (((lane & 15) ^ f) << 4) + 4096 - (i & 3) * 1024; }
    }
    int rot1 = rb + (i8 & 1) + (i8 >> 2) * 2;
    const bool shift1 = (i8 & 2) != 0;
    auto ktile = [&](int s) {
      const int plain = (s & 12) + ((s + rot1) & 3);
      const int shifted = (s < 2 || s >= 14) ? (s & 14) + ((s + rot1) & 1) : 2 + ((s - 2) & 12) + ((s - 2 + rot1) & 3);
      return ((shift1 ? shifted : plain) + 4 * hg) & 15; };
    const unsigned tstride = lw < 2 ? 128 : 64 * 8192;
    unsigned off0 = lw < 2 ? rb * 128 * 2048 : head * 256, offt = off0 + ktile(0) * tstride, lds0 = lw * 8192;
    asm volatile("" : "+s"(off0), "+s"(offt), "+s"(lds0)); SB();
    asm volatile("" : "+s"(x), "+s"(kc), "+s"(vc), "+s"(wq), "+s"(wo), "+s"(ws));
    const char* const src = (lw < 2 ? x : wq) - 4096;
    dma4(Piece{vo[0], vo[1], vo[2], vo[3], src + offt, lds0}); dma4(Piece{vo[4], vo[5], vo[6], vo[7], src + offt, lds0 + 4096});
    asm volatile("" : "+s"(rot1));
    const char* const sb = src + off0;
    auto pT = [&](int t, int half) { return Piece{vo[4 * half], vo[4 * half + 1], vo[4 * half + 2], vo[4 * half + 3], sb + ktile(t) * tstride,
                                                  t_slot(t) + lw * 8192 + half * 4096}; };
    dma4(pT(1, 0)); dma4(pT(1, 1)); dma1(pT(2, 0));
#pragma unroll
    for (int i = 0; i < 8; ++i) { const int row = i * 4 + (lane >> 4); vk[i] = row * 256 + (((lane & 15) ^ (row & 15)) << 4) + 4096 - (i & 3) * 1024; }
#pragma unroll
    for (int i = 0; i < 4; ++i) { const int f = ((lane >> 4) & 1) | ((i & 1) << 1);
      vw[i] = (lw * 32 + i * 8 + (lane >> 3)) * 2048 + (((lane & 7) ^ (f << 1)) << 4) + 4096 - i * 1024; }
    const unsigned vv0 = (lw & 1) ? vk[4] : vk[0], vv1 = (lw & 1) ? vk[5] : vk[1], vv2 = (lw & 1) ? vk[6] : vk[2], vv3 = (lw & 1) ? vk[7] : vk[3];
    const unsigned vp = lane * 16, vl4 = (lane & 31) * 4;
    const int g0 = (ct + 2 * rp) & 3, rot2 = (rp >> 1) & 3;
    auto grp = [&](int k) { return (g0 + ((ct & 2) ? ((k >> 1) | ((k & 1) << 1)) : k)) & 3; };
    auto hp = [&](int j) { return grp(j >> 2) * 4 + ((j + rot2) & 3); };
    const char* const kb = kc + (size_t)((rb * 4 + lw) * 4 + hg) * 8192;
    auto pK = [&](int half) { return Piece{vk[4 * half], vk[4 * half + 1], vk[4 * half + 2], vk[4 * half + 3], kb - 4096, L_KP + lw * 8192 + half * 4096}; };
    auto pW = [&](int j, int half) { return Piece{vw[0], vw[1], vw[2], vw[3], wo + (size_t)(hp(j) * 2 + half) * 128 * 2048 + ct * 128 - 4096,
                                                  L_W + w_slot(j) + half * 16384 + lw * 4096}; };
    auto pV = [&](int k) { return Piece{vv0, vv1, vv2, vv3, vc + (size_t)((rp * 2 + (lw >> 1)) * 4 + grp(k)) * 8192 - 4096, L_V + lw * 4096}; };
    auto pP = [&](int m) { return Piece{vp, vp, vp, vp, ws + WS_P + (size_t)((br * 16 + rp * 2 + (lw >> 1)) * 32 + hp(2 * m + (lw & 1)) * 2) * 2048,
                                        L_KP + (m & 1) * 16384 + lw * 4096}; };
    const char* const gN = ws + WS_N + (br * 4 + rb) * 128;
    const char* const gI = ws + WS_I + (br * 4 + rb) * 4096;
    const char* const myI = gI + head * 128;
    unsigned entry, nonce, nused, nfin, poll[3];
    const int slowc = (3 - (int)((size_t)wo >> 7)) & 7, tp = (rb * 2 + (i8 >> 2)) * 4 + lw;
    const int tstep = (slowc >> 2) == (hg & 1) && (i8 & 3) == 0 ? (lw < 2 ? 6 : 10) : -1;
    const char* const tptr = wo + (size_t)(((((slowc + 1 + 2 * (tp >> 4)) & 3) * 4 + ((tp >> 2) & 3)) * 256 + (tp & 3) * 64 + lane) * 2048) + ((hg & 2) * 4 + slowc) * 128;
    static_for<0, 14>([&](auto nn) {
      constexpr int n = nn.value;
      if constexpr (n >= 9 && n <= 12) { if (shift1 == (n <= 10)) wb_dma3<14>(pT(n + 2, 0)); else wb_dma3<9>(pT(n + 2, 0)); }
      else wb_dma3<(n == 1 ? 10 : 9)>(pT(n + 2, 0));
      dma4(pT(n + 2, 1));
      if constexpr (n == 8 || n == 10) { if (shift1 == (n == 8)) { touch(kb + (i8 * 8 + (lane & 7)) * 128, L_M + 2048 + lw * 256); dma4(pV(0)); } }
      if constexpr (n == 0) aload(entry, vl4, myI);
      if constexpr (n == 2) {
        entry = opaque(entry);
        if (lw == 0) {
          unsigned long long live = 0xffffffffull; unsigned nv = 0;
#pragma unroll
          for (int b = 0; b < 6; ++b) {
            const unsigned long long one = __builtin_amdgcn_ballot_w64((entry >> b) & 1u) & live, zero = live & ~one;
            const bool pick1 = __builtin_popcountll(one) < __builtin_popcountll(zero);
            nv |= pick1 ? (1u << b) : 0u; live = pick1 ? one : zero;
          }
          astore(head * 4, gN, nv);
          *(volatile LAS unsigned*)(size_t)(L_M + 16) = nv;
          asm volatile("s_waitcnt lgkmcnt(0)" ::: "memory");
        }
      }
      if constexpr (n == 4) nonce = opaque(*(volatile LAS unsigned*)(size_t)(L_M + 16));
      if constexpr (n == 6 || n == 10) { if (n == tstep) touch(tptr, L_M + 1024 + lw * 256); }
      if constexpr (n < 13) dma1(pT(n + 3, 0)); else dma1(pK(0));
    });
    wb_dma3<9>(pK(0)); dma4(pK(1)); dma1(pW(0, 0));
    wb_dma3<9>(pW(0, 0)); dma4(pW(0, 1)); aload(nused, vl4, gN); dma1(pW(1, 0));
    wb_dma3<10>(pW(1, 0)); dma4(pW(1, 1));
    wb_dma4<8>(pW(2, 0)); dma4(pW(2, 1));
    static_for<1, 16>([&](auto jj) {
      constexpr int j = jj.value, m = j - 7;
      constexpr bool hasP = m >= 0 && m < 8, hasV = j % 4 == 1 && j <= 9, hasW = j + 2 <= 15;
      constexpr int N = j == 15 ? 0 : j == 10 ? 9 : j == 14 ? 8 : 8;
      auto rest = [&]() { if constexpr (hasV) dma4(pV(j / 4 + 1)); if constexpr (hasW) { dma4(pW(j + 2, 0)); dma4(pW(j + 2, 1)); } };
      if constexpr (j == 1) {
        waitv<N>(); bar();
        if (lw == 0) astore(vl4 * 32, gI + head * 4, nused);
        rest();
      } else if constexpr (j == 6) { waitv<N>(); bar(); aload(poll[0], vl4, myI); rest(); }
      else if constexpr (j == 7 || j == 8) {
        const Piece pp = pP(m);
        const unsigned need = j == 8 ? ~0u : 3u << (2 * hp(lw & 1));
        waitv<N>(); bar(); aload(poll[m + 1], vl4, myI);
        if (__builtin_expect((differs(poll[m], nonce) & need) == 0, 1)) { dma4_dev(pp); rest(); }
        else {
          rest(); waitv<8>();
          if ((differs(poll[m + 1], nonce) & need) != 0) {
            _Pragma("unroll 1") for (unsigned spins = 0; spins < (1u << 20); ++spins) {
              unsigned f; aload(f, vl4, myI); waitv<0>();
              if ((differs(f, nonce) & need) == 0) break;
              __builtin_amdgcn_s_sleep(1);
            }
          }
          dma4_dev(pp); waitv<0>();
        }
      }
      else if constexpr (hasP) { wb_dma4_dev<N>(pP(m)); rest(); }
      else if constexpr (hasV) { wb_dma4<N>(pV(j / 4 + 1)); dma4(pW(j + 2, 0)); dma4(pW(j + 2, 1)); }
      else if constexpr (hasW) { wb_dma4<N>(pW(j + 2, 0)); dma4(pW(j + 2, 1)); }
      else { waitv<N>(); bar(); }
      if constexpr (j == 9) { keep(poll[2]); aload(nfin, vl4, gN); }
      if constexpr (j == 11) { if (lw == 0 && differs(nfin, nused) != 0) astore(vl4 * 32, gI + head * 4, nfin); }
    });
  } else {
    __builtin_amdgcn_s_setprio(1);
    const int w = wave - 4;
    const int g = lane >> 4, q4 = (lane & 15) >> 2, p = lane & 3, r = lane & 15;
    const int rh = w >> 1, dh = w & 1;
    char* pbase = a.ws + WS_P + (size_t)((br * 16 + rb * 4 + w) * 32 + head) * 2048;
    char* obase = a.out + (size_t)((rp * 2 + rh) * 32) * 4096 + br * 2048 + ct * 128 + dh * 64;
    asm volatile("" : "+s"(pbase), "+s"(obase));
    const unsigned ooff = opaque(r * 4096 + ((g & 1) * 16 + (g >> 1) * 8) * 2);
    unsigned aW[2][2], aV[4], aA[2][4], aB[2][2][2], aK[2][4];
#pragma unroll
    for (int nb = 0; nb < 2; ++nb) { aW[0][nb] = opaque(L_W + (8 * g + q4) * 128 + (((dh * 2 + nb) ^ ((q4 >> 1) | ((g & 1) << 1))) << 5) + p * 8); aW[1][nb] = opaque(aW[0][nb] + 65536); }
    const unsigned aP = opaque(L_KP + rh * 8192 + lane * 16);
#pragma unroll
    for (int s = 0; s < 4; ++s) aV[s] = opaque(L_V + (rh * 32 + r) * 256 + (((4 * s + g) ^ r) << 4));
#pragma unroll
    for (int c = 0; c < 4; ++c) { aA[0][c] = opaque(16384 + (8 * g + q4) * 256 + (((dh * 4 + c) ^ (q4 | ((g & 1) << 2))) << 5) + p * 8); aA[1][c] = opaque(aA[0][c] + 65536); }
#pragma unroll
    for (int kk = 0; kk < 2; ++kk)
#pragma unroll
      for (int o = 0; o < 2; ++o) { const int row = rh * 64 + ((o ^ dh ^ 1) << 5) + r;
        aB[0][kk][o] = opaque(row * 128 + (((4 * kk + g) ^ ((row >> 1) & 7)) << 4)); aB[1][kk][o] = opaque(aB[0][kk][o] + 65536); }
#pragma unroll
    for (int o = 0; o < 2; ++o)
#pragma unroll
      for (int i = 0; i < 4; ++i) aK[o][i] = opaque(L_KP + ((rh * 2 + (o ^ dh ^ 1)) * 32 + r) * 256 + (g & 1) * 8 + (((dh * 8 + 2 * i + (g >> 1)) ^ r) << 4));
    const unsigned aXw = opaque(L_X + w * 4096 + lane * 16), aXr = opaque(L_X + (w ^ 1) * 4096 + lane * 16);
    const unsigned aFw = opaque(L_M + w * 4), aFr = opaque(L_M + (w ^ 1) * 4);

    f4v acc[4][4];
    s4 Al[4][2], Ah[4][2]; s8 B[2][4][2];
    auto rdA = [&](auto tile, auto cc, auto ii) { constexpr int c = cc.value, kk = ii.value >> 1;
      constexpr unsigned so = t_slot(tile.value); constexpr int hi = so >= 65536; constexpr unsigned o = so - hi * 65536 + kk * 8192;
      if constexpr (ii.value & 1) Ah[c][kk] = tr(aA[hi][c] + o + 1024); else Al[c][kk] = tr(aA[hi][c] + o); };
    auto rdB = [&](auto tile, auto bb, auto kk) { constexpr int b = bb.value;
      constexpr unsigned so = t_slot(tile.value); constexpr int hi = so >= 65536; constexpr unsigned o = so - hi * 65536;
      B[tile.value & 1][b][kk.value] = rd16(aB[hi][kk.value][b >> 1] + o + (b & 1) * 2048); };
    auto rdNext = [&](auto tile, auto ii) { constexpr int i = ii.value;
      if constexpr (i < 2) rdB(tile, IC<0>{}, ii); else if constexpr (i < 6) rdA(tile, IC<0>{}, IC<i - 2>{});
      else rdB(tile, IC<(i - 4) / 2>{}, IC<i & 1>{}); };
    auto mm1 = [&](auto tile, auto cc, auto bb, auto kc) { constexpr int t = tile.value, c = cc.value, b = bb.value, kk = kc.value;
      const s8 av = cat(Al[c][kk], Ah[c][kk]), bv = B[t & 1][b][kk];
      SB();
      if constexpr (t == 0 && kk == 0) mma0(acc[c][b], av, bv);
      else if constexpr (kk == 0 && c == 0 && b == 0) mma(acc[c][b], av, bv, cat(Al[0][1], Ah[0][1]), B[t & 1][1][1]);
      else if constexpr (kk == 0 && c == 0 && b == 2) mma(acc[c][b], av, bv, B[t & 1][3][1]);
      else if constexpr (kk == 0 && c == 1 && b == 0) mma(acc[c][b], av, bv, cat(Al[1][1], Ah[1][1]));
      else if constexpr (kk == 0 && c == 2 && b == 0) mma(acc[c][b], av, bv, cat(Al[2][1], Ah[2][1]), cat(Al[3][0], Ah[3][0]), cat(Al[3][1], Ah[3][1]));
      else mma(acc[c][b], av, bv);
      SB(); };
    *(volatile LAS unsigned*)(size_t)aFw = 0u;
    barl();
    static_for<0, 12>([&](auto ii) { rdNext(IC<0>{}, ii); });
    static_for<0, 16>([&](auto t) {
      static_for<0, 16>([&](auto mm) { constexpr int m = mm.value;
        mm1(t, IC<(m >> 3)>{}, IC<((m >> 1) & 3)>{}, IC<(m & 1)>{});
        if constexpr (m < 12) rdA(t, IC<1 + (m >> 2)>{}, IC<(m & 3)>{}); });
      barl();
      if constexpr (t.value < 15) static_for<0, 16>([&](auto mm) { constexpr int m = mm.value;
        mm1(t, IC<2 + (m >> 3)>{}, IC<((m >> 1) & 3)>{}, IC<(m & 1)>{});
        if constexpr (m < 12) rdNext(IC<t.value + 1>{}, mm); });
    });
    s4 Kh[2][2][2][2];
    unsigned q[4][2][4];
    f4v St[2][2][2];
    auto rdK = [&](auto ii) { constexpr int i = ii.value, o = i >> 3, kb = (i >> 2) & 1, s = (i >> 1) & 1, u = i & 1;
      Kh[o][kb][s][u] = rd8(aK[o][s * 2 + u] + kb * 4096); };
    auto cvq = [&](auto bb, auto cc) { constexpr int b = bb.value, c = cc.value;
      q[b][c >> 1][(c & 1) * 2] = opaque(pk(acc[c][b][0], acc[c][b][1])); q[b][c >> 1][(c & 1) * 2 + 1] = opaque(pk(acc[c][b][2], acc[c][b][3])); };
    static_for<0, 16>([&](auto mm) { constexpr int m = mm.value;
      constexpr int cb[16] = {0, 0, 1, 1, 0, 0, 2, 2, 3, 3, 1, 1, 9, 2, 2, 9}, cc[16] = {0, 1, 0, 1, 2, 3, 0, 1, 0, 1, 2, 3, 0, 2, 3, 0};
      mm1(IC<15>{}, IC<2 + ((m >> 1) & 1)>{}, IC<(m >> 2)>{}, IC<(m & 1)>{});
      rdK(mm);
      if constexpr (cb[m] < 4) cvq(IC<cb[m] & 3>{}, IC<cc[m]>{}); });
    static_for<0, 16>([&](auto mm) { constexpr int m = mm.value, o = m >> 3, qb = (m >> 2) & 1, kb = (m >> 1) & 1, s = m & 1;
      const v4u qq = {q[2 * o + qb][s][0], q[2 * o + qb][s][1], q[2 * o + qb][s][2], q[2 * o + qb][s][3]};
      SB();
      if constexpr (s == 0) mma0(St[o][kb][qb], cat(Kh[o][kb][s][0], Kh[o][kb][s][1]), __builtin_bit_cast(s8, qq));
      else mma(St[o][kb][qb], cat(Kh[o][kb][s][0], Kh[o][kb][s][1]), __builtin_bit_cast(s8, qq));
      SB();
      if constexpr (m == 4 || m == 5) cvq(IC<3>{}, IC<(m - 2) & 3>{});
      if constexpr (m == 9 || m == 10 || m == 12 || m == 14) { constexpr int t = m == 9 ? 0 : m == 10 ? 1 : m == 12 ? 2 : 3;
        *(LAS f4v*)(size_t)(aXw + t * 1024) = St[0][t & 1][t >> 1]; } });
    SB();
    asm volatile("s_waitcnt lgkmcnt(0)" ::: "memory");
    *(volatile LAS unsigned*)(size_t)aFw = 1u;
    while (__builtin_amdgcn_readfirstlane(*(volatile LAS unsigned*)(size_t)aFr) == 0u) {}
    asm volatile("s_nop 3" : "+v"(St[1][0][0]), "+v"(St[1][0][1]), "+v"(St[1][1][0]), "+v"(St[1][1][1]) :: "memory");
#pragma unroll
    for (int t = 0; t < 4; ++t) St[1][t & 1][t >> 1] += *(LAS f4v*)(size_t)(aXr + t * 1024);
    const float cs = 0.08838834764831845f * 1.4426950408889634f;
    f4v e[2][2];
    float m2[2], s2[2];
#pragma unroll
    for (int qb = 0; qb < 2; ++qb) { const f4v u = St[1][0][qb], v = St[1][1][qb];
      m2[qb] = fmaxf(fmaxf(fmaxf(fmaxf(u[0], u[1]), u[2]), fmaxf(fmaxf(u[3], v[0]), v[1])), fmaxf(v[2], v[3])); }
    v2u t0 = __builtin_amdgcn_permlane16_swap(__float_as_uint(m2[0]), __float_as_uint(m2[1]), false, false);
    unsigned z = vmax(t0[0], t0[1]);
    t0 = __builtin_amdgcn_permlane32_swap(z, z, false, false);
    z = vmax(t0[0], t0[1]);
    t0 = __builtin_amdgcn_permlane16_swap(z, z, false, false);
#pragma unroll
    for (int qb = 0; qb < 2; ++qb) {
#pragma unroll
      for (int kb = 0; kb < 2; ++kb) {
        const f4v y = (St[1][kb][qb] - __uint_as_float(t0[qb])) * cs;
#pragma unroll
        for (int i = 0; i < 4; ++i) e[qb][kb][i] = __builtin_amdgcn_exp2f(y[i]);
      }
      const f4v sm = e[qb][0] + e[qb][1];
      s2[qb] = (sm[0] + sm[2]) + (sm[1] + sm[3]);
    }
    t0 = __builtin_amdgcn_permlane16_swap(__float_as_uint(s2[0]), __float_as_uint(s2[1]), false, false);
    z = __float_as_uint(__uint_as_float(t0[0]) + __uint_as_float(t0[1]));
    t0 = __builtin_amdgcn_permlane32_swap(z, z, false, false);
    z = __float_as_uint(__builtin_amdgcn_rcpf(__uint_as_float(t0[0]) + __uint_as_float(t0[1])));
    t0 = __builtin_amdgcn_permlane16_swap(z, z, false, false);
    const __amdgpu_buffer_rsrc_t prs = __builtin_amdgcn_make_buffer_rsrc(pbase, 0, 2048, 0x00020000);
#pragma unroll
    for (int qb = 0; qb < 2; ++qb) {
      const f4v u = e[qb][0] * __uint_as_float(t0[qb]), v = e[qb][1] * __uint_as_float(t0[qb]);
      const v4u pf = {pk(u[0], u[1]), pk(u[2], u[3]), pk(v[0], v[1]), pk(v[2], v[3])};
      __builtin_amdgcn_raw_buffer_store_b128(pf, prs, lane * 16 + qb * 1024, 0, 16);
    }

    s8 Vf[2][4];
    s4 Wl[4][4], Wh[4][4];
    f4v X[2][2];
    unsigned Xb[32][2][4];
    s8 Pf[4][2];
    f4v out[2][2];
    auto rdW = [&](auto rr) { constexpr int rho = rr.value, u = rho >> 3, s = (rho >> 1) & 3;
      constexpr unsigned so = w_slot(u >> 2); constexpr int hi = so >= 65536;
      constexpr unsigned o = so - hi * 65536 + ((u >> 1) & 1) * 16384 + s * 4096 + (rho & 1) * 512;
      if constexpr (rho & 1) Wh[u & 3][s] = tr(aW[hi][u & 1] + o); else Wl[u & 3][s] = tr(aW[hi][u & 1] + o); };
    auto rdV = [&](auto kb, auto s) { Vf[kb.value][s.value] = rd16(aV[s.value] + kb.value * 4096); };
    auto rdP = [&](auto pp, auto ii) { constexpr int h = 2 * pp.value + (ii.value & 1), qb = ii.value >> 1;
      Pf[h & 3][qb] = rd16(aP + ((h >> 2) & 1) * 16384 + (h & 3) * 2048 + qb * 1024); };
    auto cvt = [&](auto uu, auto ii) { constexpr int u = uu.value, i = ii.value;
      Xb[u >> 1][u & 1][i] = opaque(pk(X[u & 1][i >> 1][(i & 1) * 2], X[u & 1][i >> 1][(i & 1) * 2 + 1])); };
    auto pvm = [&](auto pp, auto nn, auto qq, auto ee) { constexpr int h = 2 * pp.value + ee.value, nb = nn.value, qb = qq.value;
      const v4u xa = {Xb[h][nb][0], Xb[h][nb][1], Xb[h][nb][2], Xb[h][nb][3]};
      SB();
      if constexpr (h == 0) mma0(out[nb][qb], __builtin_bit_cast(s8, xa), Pf[h & 3][qb]);
      else if constexpr (nb == 0 && ee.value == 0) mma(out[nb][qb], __builtin_bit_cast(s8, xa), Pf[h & 3][qb], Pf[(h + 1) & 3][qb]);
      else mma(out[nb][qb], __builtin_bit_cast(s8, xa), Pf[h & 3][qb]);
      SB(); };
    barl();
    static_for<0, 4>([&](auto s) { rdW(IC<2 * s.value>{}); rdW(IC<2 * s.value + 1>{}); rdV(IC<0>{}, s); });
    static_for<0, 4>([&](auto s) { rdV(IC<1>{}, s); });
    rdW(IC<8>{}); rdW(IC<9>{});
    static_for<0, 64>([&](auto uu) {
      constexpr int u = uu.value;
      static_for<0, 8>([&](auto ii) {
        constexpr int i = ii.value, kb = i >> 2, s = i & 3, rho = 8 * u + i + 10;
        const s8 wv = cat(Wl[u & 3][s], Wh[u & 3][s]);
        SB();
        if constexpr (i == 0) mma0(X[u & 1][kb], Vf[kb][s], wv, cat(Wl[u & 3][1], Wh[u & 3][1]));
        else if constexpr (i == 4) mma0(X[u & 1][kb], Vf[kb][s], wv);
        else if constexpr (i == 2) mma(X[u & 1][kb], Vf[kb][s], wv, cat(Wl[u & 3][3], Wh[u & 3][3]));
        else mma(X[u & 1][kb], Vf[kb][s], wv);
        SB();
        if constexpr ((u & 3) == 3 && u < 32) {
          if constexpr (i == 1) { if constexpr (u == 3) waitv<0>();
                                  barl(); }
          constexpr int lo[8] = {0, 0, 2, 4, 5, 6, 8, 9}, n[8] = {0, 2, 2, 1, 1, 2, 1, 1};
          static_for<0, n[i]>([&](auto k) { rdW(IC<8 * (u + 1) + lo[i] + k.value>{}); });
        } else if constexpr (rho < 512 && !((u & 3) == 2 && i >= 6)) rdW(IC<rho>{});
        if constexpr (i == 7) cvt(uu, IC<0>{});
        if constexpr (u >= 1 && (i == 0 || i == 3 || i == 4)) cvt(IC<(u >= 1 ? u - 1 : 0)>{}, IC<(i == 0 ? 1 : (i - 1) & 3)>{});
        if constexpr (u == 32 && ((i & 3) == 1 || (i & 3) == 2)) rdP(IC<0>{}, IC<((i >> 2) * 2 + (i & 3) - 1) & 3>{});
        if constexpr ((u & 15) == 15 && u < 63 && i >= 2) rdV(IC<((i - 2) >> 2) & 1>{}, IC<(i - 2) & 3>{});
        if constexpr ((u & 15) == 0 && u > 0 && i < 2) rdV(IC<1>{}, IC<(i + 2) & 3>{});
      });
      if constexpr (u >= 32 && u <= 60 && (u & 1) == 0)
        static_for<0, 8>([&](auto ii) { constexpr int i = ii.value, pp = ((u - 32) / 2) & 15;
          pvm(IC<pp>{}, IC<((i >> 1) & 1)>{}, IC<(i >> 2)>{}, IC<(i & 1)>{});
          if constexpr ((u & 3) == 0 && i >= 4) rdP(IC<pp + 1>{}, IC<(i & 3)>{});
          if constexpr ((u & 3) == 2) {
            if constexpr (i == 1) barl();
            if constexpr (i >= 2 && i < 6) rdP(IC<pp + 1>{}, IC<(i - 2) & 3>{});
            if constexpr (i >= 6) rdW(IC<8 * (u + 2) + i - 6>{});
          } });
    });
    static_for<0, 4>([&](auto ii) { pvm(IC<15>{}, IC<0>{}, IC<(ii.value >> 1)>{}, IC<(ii.value & 1)>{});
      if constexpr (ii.value == 0) cvt(IC<63>{}, IC<1>{}); });
    asm volatile("s_nop 3"); SB();
    cvt(IC<63>{}, IC<2>{}); cvt(IC<63>{}, IC<3>{});
    SB(); asm volatile("s_nop 1");
    static_for<0, 4>([&](auto ii) { pvm(IC<15>{}, IC<1>{}, IC<(ii.value >> 1)>{}, IC<(ii.value & 1)>{}); });
    mfma_done(out[0][0], out[0][1], out[1][0], out[1][1]);
    const __amdgpu_buffer_rsrc_t ors = __builtin_amdgcn_make_buffer_rsrc(obase, 0, 31 * 4096 + 64, 0x00020000);
#pragma unroll
    for (int qb = 0; qb < 2; ++qb) {
      const v2u s0 = __builtin_amdgcn_permlane16_swap(pk(out[0][qb][0], out[0][qb][1]), pk(out[1][qb][0], out[1][qb][1]), false, false);
      const v2u s1 = __builtin_amdgcn_permlane16_swap(pk(out[0][qb][2], out[0][qb][3]), pk(out[1][qb][2], out[1][qb][3]), false, false);
      const v4u o4 = {s0[0], s1[0], s0[1], s1[1]};
      __builtin_amdgcn_raw_buffer_store_b128(o4, ors, ooff + qb * 16 * 4096, 0, 18);
    }
    __builtin_amdgcn_endpgm();
  }
#endif
}

extern "C" void kernel_launch(void* const* d_in, const int* in_sizes, int n_in, void* d_out, int out_size, void* d_ws, size_t ws_size,
                              hipStream_t stream) {
  Args a;
  for (int i = 0; i < 10; ++i) a.in[i / 5][i % 5] = (const char*)d_in[i];
  a.out = (char*)d_out; a.ws = (char*)d_ws;
  (void)hipFuncSetAttribute((const void*)gqa_fused_kernel, hipFuncAttributeMaxDynamicSharedMemorySize, LDS_BYTES);
  gqa_fused_kernel<<<256, 512, LDS_BYTES, stream>>>(a);
}
```

```cpp
#include <hip/hip_runtime.h>
#include <hip/hip_bf16.h>
#include <stdint.h>

typedef unsigned v4u __attribute__((ext_vector_type(4)));
typedef unsigned v2u __attribute__((ext_vector_type(2)));
typedef short s4 __attribute__((ext_vector_type(4)));
typedef short s8 __attribute__((ext_vector_type(8)));
typedef __bf16 bf2 __attribute__((ext_vector_type(2)));
typedef float f4v __attribute__((ext_vector_type(4)));
typedef float f2v __attribute__((ext_vector_type(2)));
#define LAS __attribute__((address_space(3)))
#define DI __device__ __forceinline__
#define SB() __builtin_amdgcn_sched_barrier(0)

constexpr unsigned L_KP = 0, L_W = 32768;
constexpr unsigned L_X = 98304;
constexpr unsigned L_V = 131072;
constexpr unsigned L_M = 147456;
constexpr unsigned LDS_BYTES = 163840;
constexpr unsigned t_slot(int t) { return (unsigned)(t & 3) * 32768u; }
constexpr unsigned w_slot(int j) { return (unsigned)(j % 3) * 32768u; }
constexpr size_t WS_N = 0, WS_I = 4096, WS_P = 65536;

template <int N> struct IC { static constexpr int value = N; };
template <int I, int N, class F> DI void static_for(F&& f) { if constexpr (I < N) { f(IC<I>{}); static_for<I + 1, N>(f); } }

struct Piece { unsigned v0, v1, v2, v3; const char* sb; unsigned lds; };
#define GL4(P) "global_load_lds_dwordx4 %0, %4" P "\n\tglobal_load_lds_dwordx4 %1, %4 offset:1024" P "\n\t" \
               "global_load_lds_dwordx4 %2, %4 offset:2048" P "\n\tglobal_load_lds_dwordx4 %3, %4 offset:3072" P
#define DMA_OPS "v"(p.v0), "v"(p.v1), "v"(p.v2), "v"(p.v3), "s"(p.sb), "s"(p.lds)
DI void dma4(const Piece& p) { asm volatile("s_mov_b32 m0, %5\n\ts_nop 0\n\t" GL4("") :: DMA_OPS : "memory"); }
DI void dma4_dev(const Piece& p) { asm volatile("s_mov_b32 m0, %5\n\ts_nop 0\n\t" GL4(" sc1") :: DMA_OPS : "memory"); }
template <int N> DI void wb_dma4(const Piece& p) { asm volatile("s_mov_b32 m0, %5\n\ts_waitcnt vmcnt(%6)\n\ts_barrier\n\t" GL4("") :: DMA_OPS, "i"(N) : "memory"); }
template <int N> DI void wb_dma4_dev(const Piece& p) { asm volatile("s_mov_b32 m0, %5\n\ts_waitcnt vmcnt(%6)\n\ts_barrier\n\t" GL4(" sc1") :: DMA_OPS, "i"(N) : "memory"); }
DI void dma1(const Piece& p) { asm volatile("s_mov_b32 m0, %2\n\ts_nop 0\n\tglobal_load_lds_dwordx4 %0, %1" :: "v"(p.v0), "s"(p.sb), "s"(p.lds) : "memory"); }
template <int N> DI void wb_dma3(const Piece& p) {
  asm volatile("s_mov_b32 m0, %4\n\ts_waitcnt vmcnt(%5)\n\ts_barrier\n\tglobal_load_lds_dwordx4 %0, %3 offset:1024\n\t"
               "global_load_lds_dwordx4 %1, %3 offset:2048\n\tglobal_load_lds_dwordx4 %2, %3 offset:3072"
               :: "v"(p.v1), "v"(p.v2), "v"(p.v3), "s"(p.sb), "s"(p.lds), "i"(N) : "memory");
}
DI void touch(const char* p, unsigned lds) { asm volatile("s_mov_b32 m0, %1\n\ts_nop 0\n\tglobal_load_lds_dword %0, off" :: "v"(p), "s"(lds) : "memory"); }
template <int N> DI void waitv() { asm volatile("s_waitcnt vmcnt(%0)" :: "i"(N) : "memory"); }
DI void bar() { asm volatile("s_barrier" ::: "memory"); }
DI void barl() { asm volatile("s_waitcnt lgkmcnt(0)\n\ts_barrier" ::: "memory"); }
DI void aload(unsigned& d, unsigned off, const char* base) { asm volatile("global_load_dword %0, %1, %2 sc1" : "=v"(d) : "v"(off), "s"(base) : "memory"); }
DI void astore(unsigned off, const char* base, unsigned v) { asm volatile("global_store_dword %0, %1, %2 sc1" :: "v"(off), "v"(v), "s"(base) : "memory"); }
DI unsigned differs(unsigned a, unsigned b) { unsigned long long m; asm volatile("v_cmp_ne_u32 %0, %1, %2" : "=s"(m) : "v"(a), "v"(b) : "memory"); return (unsigned)m; }
DI void keep(unsigned x) { asm volatile("" :: "v"(x)); }

DI s4 tr(unsigned a) { return __builtin_amdgcn_ds_read_tr16_b64_v4i16((LAS s4*)(size_t)a); }
DI s8 cat(s4 lo, s4 hi) { return __builtin_shufflevector(lo, hi, 0, 1, 2, 3, 4, 5, 6, 7); }
DI s8 rd16(unsigned a) { return *(LAS s8*)(size_t)a; }
DI s4 rd8(unsigned a) { return *(LAS s4*)(size_t)a; }
#define MF "v_mfma_f32_16x16x32_bf16 %0, %1, %2, "
DI void mma(f4v& c, s8 a, s8 b) { asm volatile(MF "%0" : "+v"(c) : "v"(a), "v"(b)); }
DI void mma(f4v& c, s8 a, s8 b, s8 e0) { asm volatile(MF "%0" : "+v"(c) : "v"(a), "v"(b), "v"(e0)); }
DI void mma(f4v& c, s8 a, s8 b, s8 e0, s8 e1) { asm volatile(MF "%0" : "+v"(c) : "v"(a), "v"(b), "v"(e0), "v"(e1)); }
DI void mma(f4v& c, s8 a, s8 b, s8 e0, s8 e1, s8 e2) { asm volatile(MF "%0" : "+v"(c) : "v"(a), "v"(b), "v"(e0), "v"(e1), "v"(e2)); }
DI void mma0(f4v& c, s8 a, s8 b) { asm volatile(MF "0" : "=&v"(c) : "v"(a), "v"(b)); }
DI void mma0(f4v& c, s8 a, s8 b, s8 e0) { asm volatile(MF "0" : "=&v"(c) : "v"(a), "v"(b), "v"(e0)); }
DI void mfma_done(f4v& a, f4v& b, f4v& c, f4v& d) { asm volatile("s_nop 7\n\ts_nop 1" : "+v"(a), "+v"(b), "+v"(c), "+v"(d)); }
DI unsigned pk(float a, float b) { f2v v = {a, b}; return __builtin_bit_cast(unsigned, __builtin_convertvector(v, bf2)); }
DI unsigned opaque(unsigned x) { asm volatile("" : "+v"(x)); return x; }
DI unsigned vmax(unsigned a, unsigned b) { unsigned r; asm("v_max_f32 %0, %1, %2" : "=v"(r) : "v"(a), "v"(b)); return r; }

struct Args {
  const char* in[2][5];
  char* out; char* ws;
};

__global__ __launch_bounds__(512, 2) void gqa_fused_kernel(Args a) {
#if __HIP_DEVICE_COMPILE__
  const int lane = threadIdx.x & 63;
  const int wave = __builtin_amdgcn_readfirstlane(threadIdx.x >> 6);
  const int xcd = blockIdx.x & 7, idx = blockIdx.x >> 3;
  const int br = xcd >> 2, hg = xcd & 3, i8 = idx & 7, rb = idx >> 3;
  const int head = hg * 8 + i8;
  const int ct = hg * 4 + (i8 & 3), rp = rb * 2 + (i8 >> 2);

  if (__builtin_expect(wave < 4, 1)) {
    const int lw = wave;
    const char* x = a.in[br][0]; const char* kc = a.in[br][1]; const char* vc = a.in[br][2]; const char* wq = a.in[br][3]; const char* wo = a.in[br][4];
    char* ws = a.ws;
    unsigned vo[8], vk[8], vw[4];
    if (lw < 2) {
#pragma unroll
      for (int i = 0; i < 8; ++i) { const int row = lw * 64 + i * 8 + (lane >> 3); vo[i] = row * 2048 + (((lane & 7) ^ ((row >> 1) & 7)) << 4) + 4096 - (i & 3) * 1024; }
    } else {
#pragma unroll
      for (int i = 0; i < 8; ++i) { const int k = (lw - 2) * 32 + i * 4 + (lane >> 4); const int f = ((k & 3) | (((k >> 3) & 1) << 2)) << 1;
        vo[i] = k * 8192 + (((lane & 15) ^ f) << 4) + 4096 - (i & 3) * 1024; }
    }
    int rot1 = rb + (i8 & 1) + (i8 >> 2) * 2;
    const bool shift1 = (i8 & 2) != 0;
    auto ktile = [&](int s) {
      const int plain = (s & 12) + ((s + rot1) & 3);
      const int shifted = (s < 2 || s >= 14) ? (s & 14) + ((s + rot1) & 1) : 2 + ((s - 2) & 12) + ((s - 2 + rot1) & 3);
      return ((shift1 ? shifted : plain) + 4 * hg) & 15; };
    const unsigned tstride = lw < 2 ? 128 : 64 * 8192;
    unsigned off0 = lw < 2 ? rb * 128 * 2048 : head * 256, offt = off0 + ktile(0) * tstride, lds0 = lw * 8192;
    asm volatile("" : "+s"(off0), "+s"(offt), "+s"(lds0)); SB();
    asm volatile("" : "+s"(x), "+s"(kc), "+s"(vc), "+s"(wq), "+s"(wo), "+s"(ws));
    const char* const src = (lw < 2 ? x : wq) - 4096;
    dma4(Piece{vo[0], vo[1], vo[2], vo[3], src + offt, lds0}); dma4(Piece{vo[4], vo[5], vo[6], vo[7], src + offt, lds0 + 4096});
    asm volatile("" : "+s"(rot1));
    const char* const sb = src + off0;
    auto pT = [&](int t, int half) { return Piece{vo[4 * half], vo[4 * half + 1], vo[4 * half + 2], vo[4 * half + 3], sb + ktile(t) * tstride,
                                                  t_slot(t) + lw * 8192 + half * 4096}; };
    dma4(pT(1, 0)); dma4(pT(1, 1)); dma1(pT(2, 0));
#pragma unroll
    for (int i = 0; i < 8; ++i) { const int row = i * 4 + (lane >> 4); vk[i] = row * 256 + (((lane & 15) ^ (row & 15)) << 4) + 4096 - (i & 3) * 1024; }
#pragma unroll
    for (int i = 0; i < 4; ++i) { const int f = ((lane >> 4) & 1) | ((i & 1) << 1);
      vw[i] = (lw * 32 + i * 8 + (lane >> 3)) * 2048 + (((lane & 7) ^ (f << 1)) << 4) + 4096 - i * 1024; }
    const unsigned vv0 = (lw & 1) ? vk[4] : vk[0], vv1 = (lw & 1) ? vk[5] : vk[1], vv2 = (lw & 1) ? vk[6] : vk[2], vv3 = (lw & 1) ? vk[7] : vk[3];
    const unsigned vp = lane * 16, vl4 = (lane & 31) * 4;
    const int g0 = (ct + 2 * rp) & 3, rot2 = (rp >> 1) & 3;
    auto grp = [&](int k) { return (g0 + ((ct & 2) ? ((k >> 1) | ((k & 1) << 1)) : k)) & 3; };
    auto hp = [&](int j) { return grp(j >> 2) * 4 + ((j + rot2) & 3); };
    const char* const kb = kc + (size_t)((rb * 4 + lw) * 4 + hg) * 8192;
    auto pK = [&](int half) { return Piece{vk[4 * half], vk[4 * half + 1], vk[4 * half + 2], vk[4 * half + 3], kb - 4096, L_KP + lw * 8192 + half * 4096}; };
    auto pW = [&](int j, int half) { return Piece{vw[0], vw[1], vw[2], vw[3], wo + (size_t)(hp(j) * 2 + half) * 128 * 2048 + ct * 128 - 4096,
                                                  L_W + w_slot(j) + half * 16384 + lw * 4096}; };
    auto pV = [&](int k) { return Piece{vv0, vv1, vv2, vv3, vc + (size_t)((rp * 2 + (lw >> 1)) * 4 + grp(k)) * 8192 - 4096, L_V + lw * 4096}; };
    auto pP = [&](int m) { return Piece{vp, vp, vp, vp, ws + WS_P + (size_t)((br * 16 + rp * 2 + (lw >> 1)) * 32 + hp(2 * m + (lw & 1)) * 2) * 2048,
                                        L_KP + (m & 1) * 16384 + lw * 4096}; };
    const char* const gN = ws + WS_N + (br * 4 + rb) * 128;
    const char* const gI = ws + WS_I + (br * 4 + rb) * 4096;
    const char* const myI = gI + head * 128;
    unsigned entry, nonce, nused, nfin, poll[3];
    const int slowc = (3 - (int)((size_t)wo >> 7)) & 7, tp = (rb * 2 + (i8 >> 2)) * 4 + lw;
    const int tstep = (slowc >> 2) == (hg & 1) && (i8 & 3) == 0 ? (lw < 2 ? 6 : 10) : -1;
    const char* const tptr = wo + (size_t)(((((slowc + 1 + 2 * (tp >> 4)) & 3) * 4 + ((tp >> 2) & 3)) * 256 + (tp & 3) * 64 + lane) * 2048) + ((hg & 2) * 4 + slowc) * 128;
    static_for<0, 14>([&](auto nn) {
      constexpr int n = nn.value;
      if constexpr (n >= 9 && n <= 12) { if (shift1 == (n <= 10)) wb_dma3<14>(pT(n + 2, 0)); else wb_dma3<9>(pT(n + 2, 0)); }
      else wb_dma3<(n == 1 ? 10 : 9)>(pT(n + 2, 0));
      dma4(pT(n + 2, 1));
      if constexpr (n == 8 || n == 10) { if (shift1 == (n == 8)) { touch(kb + (i8 * 8 + (lane & 7)) * 128, L_M + 2048 + lw * 256); dma4(pV(0)); } }
      if constexpr (n == 0) aload(entry, vl4, myI);
      if constexpr (n == 2) {
        entry = opaque(entry);
        if (lw == 0) {
          unsigned long long live = 0xffffffffull; unsigned nv = 0;
#pragma unroll
          for (int b = 0; b < 6; ++b) {
            const unsigned long long one = __builtin_amdgcn_ballot_w64((entry >> b) & 1u) & live, zero = live & ~one;
            const bool pick1 = __builtin_popcountll(one) < __builtin_popcountll(zero);
            nv |= pick1 ? (1u << b) : 0u; live = pick1 ? one : zero;
          }
          astore(head * 4, gN, nv);
          *(volatile LAS unsigned*)(size_t)(L_M + 16) = nv;
          asm volatile("s_waitcnt lgkmcnt(0)" ::: "memory");
        }
      }
      if constexpr (n == 4) nonce = opaque(*(volatile LAS unsigned*)(size_t)(L_M + 16));
      if constexpr (n == 6 || n == 10) { if (n == tstep) touch(tptr, L_M + 1024 + lw * 256); }
      if constexpr (n < 13) dma1(pT(n + 3, 0)); else dma1(pK(0));
    });
    wb_dma3<9>(pK(0)); dma4(pK(1)); dma1(pW(0, 0));
    wb_dma3<9>(pW(0, 0)); dma4(pW(0, 1)); aload(nused, vl4, gN); dma1(pW(1, 0));
    wb_dma3<10>(pW(1, 0)); dma4(pW(1, 1));
    wb_dma4<8>(pW(2, 0)); dma4(pW(2, 1));
    static_for<1, 16>([&](auto jj) {
      constexpr int j = jj.value, m = j - 7;
      constexpr bool hasP = m >= 0 && m < 8, hasV = j % 4 == 1 && j <= 9, hasW = j + 2 <= 15;
      constexpr int N = j == 15 ? 0 : j == 10 ? 9 : 8;
      auto rest = [&]() { if constexpr (hasV) dma4(pV(j / 4 + 1)); if constexpr (hasW) { dma4(pW(j + 2, 0)); dma4(pW(j + 2, 1)); } };
      if constexpr (j == 1) {
        waitv<N>(); bar();
        if (lw == 0) astore(vl4 * 32, gI + head * 4, nused);
        rest();
      } else if constexpr (j == 6) { waitv<N>(); bar(); aload(poll[0], vl4, myI); rest(); }
      else if constexpr (j == 7 || j == 8) {
        const Piece pp = pP(m);
        const unsigned need = j == 8 ? ~0u : 3u << (2 * hp(lw & 1));
        waitv<N>(); bar(); aload(poll[m + 1], vl4, myI);
        if (__builtin_expect((differs(poll[m], nonce) & need) == 0, 1)) { dma4_dev(pp); rest(); }
        else {
          rest(); waitv<8>();
          if ((differs(poll[m + 1], nonce) & need) != 0) {
            _Pragma("unroll 1") for (unsigned spins = 0; spins < (1u << 20); ++spins) {
              unsigned f; aload(f, vl4, myI); waitv<0>();
              if ((differs(f, nonce) & need) == 0) break;
              __builtin_amdgcn_s_sleep(1);
            }
          }
          dma4_dev(pp); waitv<0>();
        }
      }
      else if constexpr (hasP) { wb_dma4_dev<N>(pP(m)); rest(); }
      else if constexpr (hasV) { wb_dma4<N>(pV(j / 4 + 1)); dma4(pW(j + 2, 0)); dma4(pW(j + 2, 1)); }
      else if constexpr (hasW) { wb_dma4<N>(pW(j + 2, 0)); dma4(pW(j + 2, 1)); }
      else { waitv<N>(); bar(); }
      if constexpr (j == 9) { keep(poll[2]); aload(nfin, vl4, gN); }
      if constexpr (j == 11) { if (lw == 0 && differs(nfin, nused) != 0) astore(vl4 * 32, gI + head * 4, nfin); }
    });
  } else {
    __builtin_amdgcn_s_setprio(1);
    const int w = wave - 4;
    const int g = lane >> 4, q4 = (lane & 15) >> 2, p = lane & 3, r = lane & 15;
    const int rh = w >> 1, dh = w & 1;
    char* pbase = a.ws + WS_P + (size_t)((br * 16 + rb * 4 + w) * 32 + head) * 2048;
    char* obase = a.out + (size_t)((rp * 2 + rh) * 32) * 4096 + br * 2048 + ct * 128 + dh * 64;
    asm volatile("" : "+s"(pbase), "+s"(obase));
    const unsigned ooff = opaque(r * 4096 + ((g & 1) * 16 + (g >> 1) * 8) * 2);
    unsigned aW[2][2], aV[4], aA[2][4], aB[2][2][2], aK[2][4];
#pragma unroll
    for (int nb = 0; nb < 2; ++nb) { aW[0][nb] = opaque(L_W + (8 * g + q4) * 128 + (((dh * 2 + nb) ^ ((q4 >> 1) | ((g & 1) << 1))) << 5) + p * 8); aW[1][nb] = opaque(aW[0][nb] + 65536); }
    const unsigned aP = opaque(L_KP + rh * 8192 + lane * 16);
#pragma unroll
    for (int s = 0; s < 4; ++s) aV[s] = opaque(L_V + (rh * 32 + r) * 256 + (((4 * s + g) ^ r) << 4));
#pragma unroll
    for (int c = 0; c < 4; ++c) { aA[0][c] = opaque(16384 + (8 * g + q4) * 256 + (((dh * 4 + c) ^ (q4 | ((g & 1) << 2))) << 5) + p * 8); aA[1][c] = opaque(aA[0][c] + 65536); }
#pragma unroll
    for (int kk = 0; kk < 2; ++kk)
#pragma unroll
      for (int o = 0; o < 2; ++o) { const int row = rh * 64 + ((o ^ dh ^ 1) << 5) + r;
        aB[0][kk][o] = opaque(row * 128 + (((4 * kk + g) ^ ((row >> 1) & 7)) << 4)); aB[1][kk][o] = opaque(aB[0][kk][o] + 65536); }
#pragma unroll
    for (int o = 0; o < 2; ++o)
#pragma unroll
      for (int i = 0; i < 4; ++i) aK[o][i] = opaque(L_KP + ((rh * 2 + (o ^ dh ^ 1)) * 32 + r) * 256 + (g & 1) * 8 + (((dh * 8 + 2 * i + (g >> 1)) ^ r) << 4));
    const unsigned aXw = opaque(L_X + w * 4096 + lane * 16), aXr = opaque(L_X + (w ^ 1) * 4096 + lane * 16);
    const unsigned aFw = opaque(L_M + w * 4), aFr = opaque(L_M + (w ^ 1) * 4);

    f4v acc[4][4];
    s4 Al[4][2], Ah[4][2]; s8 B[2][4][2];
    auto rdA = [&](auto tile, auto cc, auto ii) { constexpr int c = cc.value, kk = ii.value >> 1;
      constexpr unsigned so = t_slot(tile.value); constexpr int hi = so >= 65536; constexpr unsigned o = so - hi * 65536 + kk * 8192;
      if constexpr (ii.value & 1) Ah[c][kk] = tr(aA[hi][c] + o + 1024); else Al[c][kk] = tr(aA[hi][c] + o); };
    auto rdB = [&](auto tile, auto bb, auto kk) { constexpr int b = bb.value;
      constexpr unsigned so = t_slot(tile.value); constexpr int hi = so >= 65536; constexpr unsigned o = so - hi * 65536;
      B[tile.value & 1][b][kk.value] = rd16(aB[hi][kk.value][b >> 1] + o + (b & 1) * 2048); };
    auto rdNext = [&](auto tile, auto ii) { constexpr int i = ii.value;
      if constexpr (i < 2) rdB(tile, IC<0>{}, ii); else if constexpr (i < 6) rdA(tile, IC<0>{}, IC<i - 2>{});
      else rdB(tile, IC<(i - 4) / 2>{}, IC<i & 1>{}); };
    auto mm1 = [&](auto tile, auto cc, auto bb, auto kc) { constexpr int t = tile.value, c = cc.value, b = bb.value, kk = kc.value;
      const s8 av = cat(Al[c][kk], Ah[c][kk]), bv = B[t & 1][b][kk];
      SB();
      if constexpr (t == 0 && kk == 0) mma0(acc[c][b], av, bv);
      else if constexpr (kk == 0 && c == 0 && b == 0) mma(acc[c][b], av, bv, cat(Al[0][1], Ah[0][1]), B[t & 1][1][1]);
      else if constexpr (kk == 0 && c == 0 && b == 2) mma(acc[c][b], av, bv, B[t & 1][3][1]);
      else if constexpr (kk == 0 && c == 1 && b == 0) mma(acc[c][b], av, bv, cat(Al[1][1], Ah[1][1]));
      else if constexpr (kk == 0 && c == 2 && b == 0) mma(acc[c][b], av, bv, cat(Al[2][1], Ah[2][1]), cat(Al[3][0], Ah[3][0]), cat(Al[3][1], Ah[3][1]));
      else mma(acc[c][b], av, bv);
      SB(); };
    *(volatile LAS unsigned*)(size_t)aFw = 0u;
    barl();
    static_for<0, 12>([&](auto ii) { rdNext(IC<0>{}, ii); });
    static_for<0, 16>([&](auto t) {
      static_for<0, 16>([&](auto mm) { constexpr int m = mm.value;
        mm1(t, IC<(m >> 3)>{}, IC<((m >> 1) & 3)>{}, IC<(m & 1)>{});
        if constexpr (m < 12) rdA(t, IC<1 + (m >> 2)>{}, IC<(m & 3)>{}); });
      barl();
      if constexpr (t.value < 15) static_for<0, 16>([&](auto mm) { constexpr int m = mm.value;
        mm1(t, IC<2 + (m >> 3)>{}, IC<((m >> 1) & 3)>{}, IC<(m & 1)>{});
        if constexpr (m < 12) rdNext(IC<t.value + 1>{}, mm); });
    });
    s4 Kh[2][2][2][2];
    unsigned q[4][2][4];
    f4v St[2][2][2];
    auto rdK = [&](auto ii) { constexpr int i = ii.value, o = i >> 3, kb = (i >> 2) & 1, s = (i >> 1) & 1, u = i & 1;
      Kh[o][kb][s][u] = rd8(aK[o][s * 2 + u] + kb * 4096); };
    auto cvq = [&](auto bb, auto cc) { constexpr int b = bb.value, c = cc.value;
      q[b][c >> 1][(c & 1) * 2] = opaque(pk(acc[c][b][0], acc[c][b][1])); q[b][c >> 1][(c & 1) * 2 + 1] = opaque(pk(acc[c][b][2], acc[c][b][3])); };
    static_for<0, 16>([&](auto mm) { constexpr int m = mm.value;
      constexpr int cb[16] = {0, 0, 1, 1, 0, 0, 2, 2, 3, 3, 1, 1, 9, 2, 2, 9}, cc[16] = {0, 1, 0, 1, 2, 3, 0, 1, 0, 1, 2, 3, 0, 2, 3, 0};
      mm1(IC<15>{}, IC<2 + ((m >> 1) & 1)>{}, IC<(m >> 2)>{}, IC<(m & 1)>{});
      rdK(mm);
      if constexpr (cb[m] < 4) cvq(IC<cb[m] & 3>{}, IC<cc[m]>{}); });
    static_for<0, 16>([&](auto mm) { constexpr int m = mm.value, o = m >> 3, qb = (m >> 2) & 1, kb = (m >> 1) & 1, s = m & 1;
      const v4u qq = {q[2 * o + qb][s][0], q[2 * o + qb][s][1], q[2 * o + qb][s][2], q[2 * o + qb][s][3]};
      SB();
      if constexpr (s == 0) mma0(St[o][kb][qb], cat(Kh[o][kb][s][0], Kh[o][kb][s][1]), __builtin_bit_cast(s8, qq));
      else mma(St[o][kb][qb], cat(Kh[o][kb][s][0], Kh[o][kb][s][1]), __builtin_bit_cast(s8, qq));
      SB();
      if constexpr (m == 4 || m == 5) cvq(IC<3>{}, IC<(m - 2) & 3>{});
      if constexpr (m == 9 || m == 10 || m == 12 || m == 14) { constexpr int t = m == 9 ? 0 : m == 10 ? 1 : m == 12 ? 2 : 3;
        *(LAS f4v*)(size_t)(aXw + t * 1024) = St[0][t & 1][t >> 1]; } });
    SB();
    asm volatile("s_waitcnt lgkmcnt(0)" ::: "memory");
    *(volatile LAS unsigned*)(size_t)aFw = 1u;
    while (__builtin_amdgcn_readfirstlane(*(volatile LAS unsigned*)(size_t)aFr) == 0u) {}
    asm volatile("s_nop 3" : "+v"(St[1][0][0]), "+v"(St[1][0][1]), "+v"(St[1][1][0]), "+v"(St[1][1][1]) :: "memory");
    f4v xr[4];
#pragma unroll
    for (int t = 0; t < 4; ++t) xr[t] = *(LAS f4v*)(size_t)(aXr + t * 1024);
    s8 Vf[2][4];
    auto rdV = [&](auto kb, auto s) { Vf[kb.value][s.value] = rd16(aV[s.value] + kb.value * 4096); };
    SB();
    static_for<0, 8>([&](auto i) { rdV(IC<(i.value >> 2)>{}, IC<(i.value & 3)>{}); });
    SB();
#pragma unroll
    for (int t = 0; t < 4; ++t) St[1][t & 1][t >> 1] += xr[t];
    const float cs = 0.08838834764831845f * 1.4426950408889634f;
    f4v e[2][2];
    float m2[2], s2[2];
#pragma unroll
    for (int qb = 0; qb < 2; ++qb) { const f4v u = St[1][0][qb], v = St[1][1][qb];
      m2[qb] = fmaxf(fmaxf(fmaxf(fmaxf(u[0], u[1]), u[2]), fmaxf(fmaxf(u[3], v[0]), v[1])), fmaxf(v[2], v[3])); }
    v2u t0 = __builtin_amdgcn_permlane16_swap(__float_as_uint(m2[0]), __float_as_uint(m2[1]), false, false);
    unsigned z = vmax(t0[0], t0[1]);
    t0 = __builtin_amdgcn_permlane32_swap(z, z, false, false);
    z = vmax(t0[0], t0[1]);
    t0 = __builtin_amdgcn_permlane16_swap(z, z, false, false);
#pragma unroll
    for (int qb = 0; qb < 2; ++qb) {
#pragma unroll
      for (int kb = 0; kb < 2; ++kb) {
        const f4v y = (St[1][kb][qb] - __uint_as_float(t0[qb])) * cs;
#pragma unroll
        for (int i = 0; i < 4; ++i) e[qb][kb][i] = __builtin_amdgcn_exp2f(y[i]);
      }
      const f4v sm = e[qb][0] + e[qb][1];
      s2[qb] = (sm[0] + sm[2]) + (sm[1] + sm[3]);
    }
    t0 = __builtin_amdgcn_permlane16_swap(__float_as_uint(s2[0]), __float_as_uint(s2[1]), false, false);
    z = __float_as_uint(__uint_as_float(t0[0]) + __uint_as_float(t0[1]));
    t0 = __builtin_amdgcn_permlane32_swap(z, z, false, false);
    z = __float_as_uint(__builtin_amdgcn_rcpf(__uint_as_float(t0[0]) + __uint_as_float(t0[1])));
    t0 = __builtin_amdgcn_permlane16_swap(z, z, false, false);
    const __amdgpu_buffer_rsrc_t prs = __builtin_amdgcn_make_buffer_rsrc(pbase, 0, 2048, 0x00020000);
#pragma unroll
    for (int qb = 0; qb < 2; ++qb) {
      const f4v u = e[qb][0] * __uint_as_float(t0[qb]), v = e[qb][1] * __uint_as_float(t0[qb]);
      const v4u pf = {pk(u[0], u[1]), pk(u[2], u[3]), pk(v[0], v[1]), pk(v[2], v[3])};
      __builtin_amdgcn_raw_buffer_store_b128(pf, prs, lane * 16 + qb * 1024, 0, 16);
    }

    s4 Wl[4][4], Wh[4][4];
    f4v X[2][2];
    unsigned Xb[32][2][4];
    s8 Pf[4][2];
    f4v out[2][2];
    auto rdW = [&](auto rr) { constexpr int rho = rr.value, u = rho >> 3, s = (rho >> 1) & 3;
      constexpr unsigned so = w_slot(u >> 2); constexpr int hi = so >= 65536;
      constexpr unsigned o = so - hi * 65536 + ((u >> 1) & 1) * 16384 + s * 4096 + (rho & 1) * 512;
      if constexpr (rho & 1) Wh[u & 3][s] = tr(aW[hi][u & 1] + o); else Wl[u & 3][s] = tr(aW[hi][u & 1] + o); };
    auto rdP = [&](auto pp, auto ii) { constexpr int h = 2 * pp.value + (ii.value & 1), qb = ii.value >> 1;
      Pf[h & 3][qb] = rd16(aP + ((h >> 2) & 1) * 16384 + (h & 3) * 2048 + qb * 1024); };
    auto cvt = [&](auto uu, auto ii) { constexpr int u = uu.value, i = ii.value;
      Xb[u >> 1][u & 1][i] = opaque(pk(X[u & 1][i >> 1][(i & 1) * 2], X[u & 1][i >> 1][(i & 1) * 2 + 1])); };
    auto pvm = [&](auto pp, auto nn, auto qq, auto ee) { constexpr int h = 2 * pp.value + ee.value, nb = nn.value, qb = qq.value;
      const v4u xa = {Xb[h][nb][0], Xb[h][nb][1], Xb[h][nb][2], Xb[h][nb][3]};
      SB();
      if constexpr (h == 0) mma0(out[nb][qb], __builtin_bit_cast(s8, xa), Pf[h & 3][qb]);
      else if constexpr (nb == 0 && ee.value == 0) mma(out[nb][qb], __builtin_bit_cast(s8, xa), Pf[h & 3][qb], Pf[(h + 1) & 3][qb]);
      else mma(out[nb][qb], __builtin_bit_cast(s8, xa), Pf[h & 3][qb]);
      SB(); };
    barl();
    static_for<0, 10>([&](auto i) { rdW(i); });
    static_for<0, 64>([&](auto uu) {
      constexpr int u = uu.value;
      static_for<0, 8>([&](auto ii) {
        constexpr int i = ii.value, kb = i >> 2, s = i & 3, rho = 8 * u + i + 10;
        const s8 wv = cat(Wl[u & 3][s], Wh[u & 3][s]);
        SB();
        if constexpr (i == 0) mma0(X[u & 1][kb], Vf[kb][s], wv, cat(Wl[u & 3][1], Wh[u & 3][1]));
        else if constexpr (i == 4) mma0(X[u & 1][kb], Vf[kb][s], wv);
        else if constexpr (i == 2) mma(X[u & 1][kb], Vf[kb][s], wv, cat(Wl[u & 3][3], Wh[u & 3][3]));
        else mma(X[u & 1][kb], Vf[kb][s], wv);
        SB();
        if constexpr ((u & 3) == 3 && u < 32) {
          if constexpr (i == 1) { if constexpr (u == 3) waitv<0>();
                                  barl(); }
          constexpr int lo[8] = {0, 0, 2, 4, 5, 6, 8, 9}, n[8] = {0, 2, 2, 1, 1, 2, 1, 1};
          static_for<0, n[i]>([&](auto k) { rdW(IC<8 * (u + 1) + lo[i] + k.value>{}); });
        } else if constexpr (rho < 512 && !((u & 3) == 2 && i >= 6)) rdW(IC<rho>{});
        if constexpr (i == 7) cvt(uu, IC<0>{});
        if constexpr (u >= 1 && (i == 0 || i == 3 || i == 4)) cvt(IC<(u >= 1 ? u - 1 : 0)>{}, IC<(i == 0 ? 1 : (i - 1) & 3)>{});
        if constexpr (u == 32 && ((i & 3) == 1 || (i & 3) == 2)) rdP(IC<0>{}, IC<((i >> 2) * 2 + (i & 3) - 1) & 3>{});
        if constexpr ((u & 15) == 15 && u < 63 && i >= 2) rdV(IC<((i - 2) >> 2) & 1>{}, IC<(i - 2) & 3>{});
        if constexpr ((u & 15) == 0 && u > 0 && i < 2) rdV(IC<1>{}, IC<(i + 2) & 3>{});
      });
      if constexpr (u >= 32 && u <= 60 && (u & 1) == 0)
        static_for<0, 8>([&](auto ii) { constexpr int i = ii.value, pp = ((u - 32) / 2) & 15;
          pvm(IC<pp>{}, IC<((i >> 1) & 1)>{}, IC<(i >> 2)>{}, IC<(i & 1)>{});
          if constexpr ((u & 3) == 0 && i >= 4) rdP(IC<pp + 1>{}, IC<(i & 3)>{});
          if constexpr ((u & 3) == 2) {
            if constexpr (i == 1) barl();
            if constexpr (i >= 2 && i < 6) rdP(IC<pp + 1>{}, IC<(i - 2) & 3>{});
            if constexpr (i >= 6) rdW(IC<8 * (u + 2) + i - 6>{});
          } });
    });
    static_for<0, 4>([&](auto ii) { pvm(IC<15>{}, IC<0>{}, IC<(ii.value >> 1)>{}, IC<(ii.value & 1)>{});
      if constexpr (ii.value == 0) cvt(IC<63>{}, IC<1>{}); });
    asm volatile("s_nop 3"); SB();
    cvt(IC<63>{}, IC<2>{}); cvt(IC<63>{}, IC<3>{});
    SB(); asm volatile("s_nop 1");
    static_for<0, 4>([&](auto ii) { pvm(IC<15>{}, IC<1>{}, IC<(ii.value >> 1)>{}, IC<(ii.value & 1)>{}); });
    mfma_done(out[0][0], out[0][1], out[1][0], out[1][1]);
    const __amdgpu_buffer_rsrc_t ors = __builtin_amdgcn_make_buffer_rsrc(obase, 0, 31 * 4096 + 64, 0x00020000);
#pragma unroll
    for (int qb = 0; qb < 2; ++qb) {
      const v2u s0 = __builtin_amdgcn_permlane16_swap(pk(out[0][qb][0], out[0][qb][1]), pk(out[1][qb][0], out[1][qb][1]), false, false);
      const v2u s1 = __builtin_amdgcn_permlane16_swap(pk(out[0][qb][2], out[0][qb][3]), pk(out[1][qb][2], out[1][qb][3]), false, false);
      const v4u o4 = {s0[0], s1[0], s0[1], s1[1]};
      __builtin_amdgcn_raw_buffer_store_b128(o4, ors, ooff + qb * 16 * 4096, 0, 18);
    }
    __builtin_amdgcn_endpgm();
  }
#endif
}

extern "C" void kernel_launch(void* const* d_in, const int* in_sizes, int n_in, void* d_out, int out_size, void* d_ws, size_t ws_size,
                              hipStream_t stream) {
  Args a;
  for (int i = 0; i < 10; ++i) a.in[i / 5][i % 5] = (const char*)d_in[i];
  a.out = (char*)d_out; a.ws = (char*)d_ws;
  (void)hipFuncSetAttribute((const void*)gqa_fused_kernel, hipFuncAttributeMaxDynamicSharedMemorySize, LDS_BYTES);
  gqa_fused_kernel<<<256, 512, LDS_BYTES, stream>>>(a);
}
```

```cpp
#include <hip/hip_runtime.h>
#include <hip/hip_bf16.h>
#include <stdint.h>

typedef unsigned v4u __attribute__((ext_vector_type(4)));
typedef unsigned v2u __attribute__((ext_vector_type(2)));
typedef short s4 __attribute__((ext_vector_type(4)));
typedef short s8 __attribute__((ext_vector_type(8)));
typedef __bf16 bf2 __attribute__((ext_vector_type(2)));
typedef float f4v __attribute__((ext_vector_type(4)));
typedef float f2v __attribute__((ext_vector_type(2)));
#define LAS __attribute__((address_space(3)))
#define DI __device__ __forceinline__
#define SB() __builtin_amdgcn_sched_barrier(0)

constexpr unsigned L_KP = 0, L_W = 32768;
constexpr unsigned L_X = 98304;
constexpr unsigned L_V = 131072;
constexpr unsigned L_M = 147456;
constexpr unsigned LDS_BYTES = 163840;
constexpr unsigned t_slot(int t) { return (unsigned)(t & 3) * 32768u; }
constexpr unsigned w_slot(int j) { return (unsigned)(j % 3) * 32768u; }
constexpr size_t WS_N = 0, WS_I = 4096, WS_P = 65536;

template <int N> struct IC { static constexpr int value = N; };
template <int I, int N, class F> DI void static_for(F&& f) { if constexpr (I < N) { f(IC<I>{}); static_for<I + 1, N>(f); } }

struct Piece { unsigned v0, v1, v2, v3; const char* sb; unsigned lds; };
#define GL4(P) "global_load_lds_dwordx4 %0, %4" P "\n\tglobal_load_lds_dwordx4 %1, %4 offset:1024" P "\n\t" \
               "global_load_lds_dwordx4 %2, %4 offset:2048" P "\n\tglobal_load_lds_dwordx4 %3, %4 offset:3072" P
#define DMA_OPS "v"(p.v0), "v"(p.v1), "v"(p.v2), "v"(p.v3), "s"(p.sb), "s"(p.lds)
DI void dma4(const Piece& p) { asm volatile("s_mov_b32 m0, %5\n\ts_nop 0\n\t" GL4("") :: DMA_OPS : "memory"); }
DI void dma4_dev(const Piece& p) { asm volatile("s_mov_b32 m0, %5\n\ts_nop 0\n\t" GL4(" sc1") :: DMA_OPS : "memory"); }
template <int N> DI void wb_dma4(const Piece& p) { asm volatile("s_mov_b32 m0, %5\n\ts_waitcnt vmcnt(%6)\n\ts_barrier\n\t" GL4("") :: DMA_OPS, "i"(N) : "memory"); }
template <int N> DI void wb_dma4_dev(const Piece& p) { asm volatile("s_mov_b32 m0, %5\n\ts_waitcnt vmcnt(%6)\n\ts_barrier\n\t" GL4(" sc1") :: DMA_OPS, "i"(N) : "memory"); }
DI void dma1(const Piece& p) { asm volatile("s_mov_b32 m0, %2\n\ts_nop 0\n\tglobal_load_lds_dwordx4 %0, %1" :: "v"(p.v0), "s"(p.sb), "s"(p.lds) : "memory"); }
template <int N> DI void wb_dma3(const Piece& p) {
  asm volatile("s_mov_b32 m0, %4\n\ts_waitcnt vmcnt(%5)\n\ts_barrier\n\tglobal_load_lds_dwordx4 %0, %3 offset:1024\n\t"
               "global_load_lds_dwordx4 %1, %3 offset:2048\n\tglobal_load_lds_dwordx4 %2, %3 offset:3072"
               :: "v"(p.v1), "v"(p.v2), "v"(p.v3), "s"(p.sb), "s"(p.lds), "i"(N) : "memory");
}
DI void touch(const char* p, unsigned lds) { asm volatile("s_mov_b32 m0, %1\n\ts_nop 0\n\tglobal_load_lds_dword %0, off" :: "v"(p), "s"(lds) : "memory"); }
template <int N> DI void waitv() { asm volatile("s_waitcnt vmcnt(%0)" :: "i"(N) : "memory"); }
DI void bar() { asm volatile("s_barrier" ::: "memory"); }
DI void barl() { asm volatile("s_waitcnt lgkmcnt(0)\n\ts_barrier" ::: "memory"); }
DI void aload(unsigned& d, unsigned off, const char* base) { asm volatile("global_load_dword %0, %1, %2 sc1" : "=v"(d) : "v"(off), "s"(base) : "memory"); }
DI void astore(unsigned off, const char* base, unsigned v) { asm volatile("global_store_dword %0, %1, %2 sc1" :: "v"(off), "v"(v), "s"(base) : "memory"); }
DI unsigned differs(unsigned a, unsigned b) { unsigned long long m; asm volatile("v_cmp_ne_u32 %0, %1, %2" : "=s"(m) : "v"(a), "v"(b) : "memory"); return (unsigned)m; }
DI void keep(unsigned x) { asm volatile("" :: "v"(x)); }

DI s4 tr(unsigned a) { return __builtin_amdgcn_ds_read_tr16_b64_v4i16((LAS s4*)(size_t)a); }
DI s8 cat(s4 lo, s4 hi) { return __builtin_shufflevector(lo, hi, 0, 1, 2, 3, 4, 5, 6, 7); }
DI s8 rd16(unsigned a) { return *(LAS s8*)(size_t)a; }
DI s4 rd8(unsigned a) { return *(LAS s4*)(size_t)a; }
#define MF "v_mfma_f32_16x16x32_bf16 %0, %1, %2, "
DI void mma(f4v& c, s8 a, s8 b) { asm volatile(MF "%0" : "+v"(c) : "v"(a), "v"(b)); }
DI void mma(f4v& c, s8 a, s8 b, s8 e0) { asm volatile(MF "%0" : "+v"(c) : "v"(a), "v"(b), "v"(e0)); }
DI void mma(f4v& c, s8 a, s8 b, s8 e0, s8 e1) { asm volatile(MF "%0" : "+v"(c) : "v"(a), "v"(b), "v"(e0), "v"(e1)); }
DI void mma(f4v& c, s8 a, s8 b, s8 e0, s8 e1, s8 e2) { asm volatile(MF "%0" : "+v"(c) : "v"(a), "v"(b), "v"(e0), "v"(e1), "v"(e2)); }
DI void mma0(f4v& c, s8 a, s8 b) { asm volatile(MF "0" : "=&v"(c) : "v"(a), "v"(b)); }
DI void mma0(f4v& c, s8 a, s8 b, s8 e0) { asm volatile(MF "0" : "=&v"(c) : "v"(a), "v"(b), "v"(e0)); }
DI void mfma_done(f4v& a, f4v& b, f4v& c, f4v& d) { asm volatile("s_nop 7\n\ts_nop 1" : "+v"(a), "+v"(b), "+v"(c), "+v"(d)); }
DI unsigned pk(float a, float b) { f2v v = {a, b}; return __builtin_bit_cast(unsigned, __builtin_convertvector(v, bf2)); }
DI unsigned opaque(unsigned x) { asm volatile("" : "+v"(x)); return x; }
DI unsigned vmax(unsigned a, unsigned b) { unsigned r; asm("v_max_f32 %0, %1, %2" : "=v"(r) : "v"(a), "v"(b)); return r; }

struct Args {
  const char* in[2][5];
  char* out; char* ws;
};

__global__ __launch_bounds__(512, 2) void gqa_fused_kernel(Args a) {
#if __HIP_DEVICE_COMPILE__
  const int lane = threadIdx.x & 63;
  const int wave = __builtin_amdgcn_readfirstlane(threadIdx.x >> 6);
  const int xcd = blockIdx.x & 7, idx = blockIdx.x >> 3;
  const int br = xcd >> 2, hg = xcd & 3, i8 = idx & 7, rb = idx >> 3;
  const int head = hg * 8 + i8;
  const int ct = hg * 4 + (i8 & 3), rp = rb * 2 + (i8 >> 2);

  if (__builtin_expect(wave < 4, 1)) {
    const int lw = wave;
    const char* x = a.in[br][0]; const char* kc = a.in[br][1]; const char* vc = a.in[br][2]; const char* wq = a.in[br][3]; const char* wo = a.in[br][4];
    char* ws = a.ws;
    unsigned vo[8], vk[8], vw[4];
    if (lw < 2) {
#pragma unroll
      for (int i = 0; i < 8; ++i) { const int row = lw * 64 + i * 8 + (lane >> 3); vo[i] = row * 2048 + (((lane & 7) ^ ((row >> 1) & 7)) << 4) + 4096 - (i & 3) * 1024; }
    } else {
#pragma unroll
      for (int i = 0; i < 8; ++i) { const int k = (lw - 2) * 32 + i * 4 + (lane >> 4); const int f = ((k & 3) | (((k >> 3) & 1) << 2)) << 1;
        vo[i] = k * 8192 + (((lane & 15) ^ f) << 4) + 4096 - (i & 3) * 1024; }
    }
    int rot1 = rb + (i8 & 1) + (i8 >> 2) * 2;
    const bool shift1 = (i8 & 2) != 0;
    auto ktile = [&](int s) {
      const int plain = (s & 12) + ((s + rot1) & 3);
      const int shifted = (s < 2 || s >= 14) ? (s & 14) + ((s + rot1) & 1) : 2 + ((s - 2) & 12) + ((s - 2 + rot1) & 3);
      return ((shift1 ? shifted : plain) + 4 * hg) & 15; };
    const unsigned tstride = lw < 2 ? 128 : 64 * 8192;
    unsigned off0 = lw < 2 ? rb * 128 * 2048 : head * 256, offt = off0 + ktile(0) * tstride, lds0 = lw * 8192;
    asm volatile("" : "+s"(off0), "+s"(offt), "+s"(lds0)); SB();
    asm volatile("" : "+s"(x), "+s"(kc), "+s"(vc), "+s"(wq), "+s"(wo), "+s"(ws));
    const char* const src = (lw < 2 ? x : wq) - 4096;
    dma4(Piece{vo[0], vo[1], vo[2], vo[3], src + offt, lds0}); dma4(Piece{vo[4], vo[5], vo[6], vo[7], src + offt, lds0 + 4096});
    asm volatile("" : "+s"(rot1));
    const char* const sb = src + off0;
    auto pT = [&](int t, int half) { return Piece{vo[4 * half], vo[4 * half + 1], vo[4 * half + 2], vo[4 * half + 3], sb + ktile(t) * tstride,
                                                  t_slot(t) + lw * 8192 + half * 4096}; };
    dma4(pT(1, 0)); dma4(pT(1, 1));
#pragma unroll
    for (int i = 0; i < 8; ++i) { const int row = i * 4 + (lane >> 4); vk[i] = row * 256 + (((lane & 15) ^ (row & 15)) << 4) + 4096 - (i & 3) * 1024; }
#pragma unroll
    for (int i = 0; i < 4; ++i) { const int f = ((lane >> 4) & 1) | ((i & 1) << 1);
      vw[i] = (lw * 32 + i * 8 + (lane >> 3)) * 2048 + (((lane & 7) ^ (f << 1)) << 4) + 4096 - i * 1024; }
    const unsigned vv0 = (lw & 1) ? vk[4] : vk[0], vv1 = (lw & 1) ? vk[5] : vk[1], vv2 = (lw & 1) ? vk[6] : vk[2], vv3 = (lw & 1) ? vk[7] : vk[3];
    const unsigned vp = lane * 16, vl4 = (lane & 31) * 4;
    const int g0 = (ct + 2 * rp) & 3, rot2 = (rp >> 1) & 3;
    auto grp = [&](int k) { return (g0 + ((ct & 2) ? ((k >> 1) | ((k & 1) << 1)) : k)) & 3; };
    auto hp = [&](int j) { return grp(j >> 2) * 4 + ((j + rot2) & 3); };
    const char* const kb = kc + (size_t)((rb * 4 + lw) * 4 + hg) * 8192;
    auto pK = [&](int half) { return Piece{vk[4 * half], vk[4 * half + 1], vk[4 * half + 2], vk[4 * half + 3], kb - 4096, L_KP + lw * 8192 + half * 4096}; };
    auto pW = [&](int j, int half) { return Piece{vw[0], vw[1], vw[2], vw[3], wo + (size_t)(hp(j) * 2 + half) * 128 * 2048 + ct * 128 - 4096,
                                                  L_W + w_slot(j) + half * 16384 + lw * 4096}; };
    auto pV = [&](int k) { return Piece{vv0, vv1, vv2, vv3, vc + (size_t)((rp * 2 + (lw >> 1)) * 4 + grp(k)) * 8192 - 4096, L_V + lw * 4096}; };
    auto pP = [&](int m) { return Piece{vp, vp, vp, vp, ws + WS_P + (size_t)((br * 16 + rp * 2 + (lw >> 1)) * 32 + hp(2 * m + (lw & 1)) * 2) * 2048,
                                        L_KP + (m & 1) * 16384 + lw * 4096}; };
    const char* const gN = ws + WS_N + (br * 4 + rb) * 128;
    const char* const gI = ws + WS_I + (br * 4 + rb) * 4096;
    const char* const myI = gI + head * 128;
    unsigned entry, nonce, nused, nfin, poll[3];
    const int slowc = (3 - (int)((size_t)wo >> 7)) & 7, tp = (rb * 2 + (i8 >> 2)) * 4 + lw;
    const int tstep = (slowc >> 2) == (hg & 1) && (i8 & 3) == 0 ? (lw < 2 ? 6 : 10) : -1;
    const char* const tptr = wo + (size_t)(((((slowc + 1 + 2 * (tp >> 4)) & 3) * 4 + ((tp >> 2) & 3)) * 256 + (tp & 3) * 64 + lane) * 2048) + ((hg & 2) * 4 + slowc) * 128;
    static_for<0, 14>([&](auto nn) {
      constexpr int n = nn.value;
      if constexpr (n >= 9 && n <= 12) { if (shift1 == (n <= 10)) wb_dma4<13>(pT(n + 2, 0)); else wb_dma4<8>(pT(n + 2, 0)); }
      else wb_dma4<(n == 1 ? 9 : 8)>(pT(n + 2, 0));
      dma4(pT(n + 2, 1));
      if constexpr (n == 8 || n == 10) { if (shift1 == (n == 8)) { touch(kb + (i8 * 8 + (lane & 7)) * 128, L_M + 2048 + lw * 256); dma4(pV(0)); } }
      if constexpr (n == 0) aload(entry, vl4, myI);
      if constexpr (n == 2) {
        entry = opaque(entry);
        if (lw == 0) {
          unsigned long long live = 0xffffffffull; unsigned nv = 0;
#pragma unroll
          for (int b = 0; b < 6; ++b) {
            const unsigned long long one = __builtin_amdgcn_ballot_w64((entry >> b) & 1u) & live, zero = live & ~one;
            const bool pick1 = __builtin_popcountll(one) < __builtin_popcountll(zero);
            nv |= pick1 ? (1u << b) : 0u; live = pick1 ? one : zero;
          }
          astore(head * 4, gN, nv);
          *(volatile LAS unsigned*)(size_t)(L_M + 16) = nv;
          asm volatile("s_waitcnt lgkmcnt(0)" ::: "memory");
        }
      }
      if constexpr (n == 4) nonce = opaque(*(volatile LAS unsigned*)(size_t)(L_M + 16));
      if constexpr (n == 6 || n == 10) { if (n == tstep) touch(tptr, L_M + 1024 + lw * 256); }
    });
    wb_dma4<8>(pK(0)); dma4(pK(1)); dma1(pW(0, 0));
    wb_dma3<9>(pW(0, 0)); dma4(pW(0, 1)); aload(nused, vl4, gN); dma1(pW(1, 0));
    wb_dma3<10>(pW(1, 0)); dma4(pW(1, 1));
    wb_dma4<8>(pW(2, 0)); dma4(pW(2, 1));
    static_for<1, 16>([&](auto jj) {
      constexpr int j = jj.value, m = j - 7;
      constexpr bool hasP = m >= 0 && m < 8, hasV = j % 4 == 1 && j <= 9, hasW = j + 2 <= 15;
      constexpr int N = j == 15 ? 0 : j == 10 ? 9 : 8;
      auto rest = [&]() { if constexpr (hasV) dma4(pV(j / 4 + 1)); if constexpr (hasW) { dma4(pW(j + 2, 0)); dma4(pW(j + 2, 1)); } };
      if constexpr (j == 1) {
        waitv<N>(); bar();
        if (lw == 0) astore(vl4 * 32, gI + head * 4, nused);
        rest();
      } else if constexpr (j == 6) { waitv<N>(); bar(); aload(poll[0], vl4, myI); rest(); }
      else if constexpr (j == 7 || j == 8) {
        const Piece pp = pP(m);
        const unsigned need = j == 8 ? ~0u : 3u << (2 * hp(lw & 1));
        waitv<N>(); bar(); aload(poll[m + 1], vl4, myI);
        if (__builtin_expect((differs(poll[m], nonce) & need) == 0, 1)) { dma4_dev(pp); rest(); }
        else {
          rest(); waitv<8>();
          if ((differs(poll[m + 1], nonce) & need) != 0) {
            _Pragma("unroll 1") for (unsigned spins = 0; spins < (1u << 20); ++spins) {
              unsigned f; aload(f, vl4, myI); waitv<0>();
              if ((differs(f, nonce) & need) == 0) break;
              __builtin_amdgcn_s_sleep(1);
            }
          }
          dma4_dev(pp); waitv<0>();
        }
      }
      else if constexpr (hasP) { wb_dma4_dev<N>(pP(m)); rest(); }
      else if constexpr (hasV) { wb_dma4<N>(pV(j / 4 + 1)); dma4(pW(j + 2, 0)); dma4(pW(j + 2, 1)); }
      else if constexpr (hasW) { wb_dma4<N>(pW(j + 2, 0)); dma4(pW(j + 2, 1)); }
      else { waitv<N>(); bar(); }
      if constexpr (j == 9) { keep(poll[2]); aload(nfin, vl4, gN); }
      if constexpr (j == 11) { if (lw == 0 && differs(nfin, nused) != 0) astore(vl4 * 32, gI + head * 4, nfin); }
    });
  } else {
    __builtin_amdgcn_s_setprio(1);
    const int w = wave - 4;
    const int g = lane >> 4, q4 = (lane & 15) >> 2, p = lane & 3, r = lane & 15;
    const int rh = w >> 1, dh = w & 1;
    char* pbase = a.ws + WS_P + (size_t)((br * 16 + rb * 4 + w) * 32 + head) * 2048;
    char* obase = a.out + (size_t)((rp * 2 + rh) * 32) * 4096 + br * 2048 + ct * 128 + dh * 64;
    asm volatile("" : "+s"(pbase), "+s"(obase));
    const unsigned ooff = opaque(r * 4096 + ((g & 1) * 16 + (g >> 1) * 8) * 2);
    unsigned aW[2][2], aV[4], aA[2][4], aB[2][2][2], aK[2][4];
#pragma unroll
    for (int nb = 0; nb < 2; ++nb) { aW[0][nb] = opaque(L_W + (8 * g + q4) * 128 + (((dh * 2 + nb) ^ ((q4 >> 1) | ((g & 1) << 1))) << 5) + p * 8); aW[1][nb] = opaque(aW[0][nb] + 65536); }
    const unsigned aP = opaque(L_KP + rh * 8192 + lane * 16);
#pragma unroll
    for (int s = 0; s < 4; ++s) aV[s] = opaque(L_V + (rh * 32 + r) * 256 + (((4 * s + g) ^ r) << 4));
#pragma unroll
    for (int c = 0; c < 4; ++c) { aA[0][c] = opaque(16384 + (8 * g + q4) * 256 + (((dh * 4 + c) ^ (q4 | ((g & 1) << 2))) << 5) + p * 8); aA[1][c] = opaque(aA[0][c] + 65536); }
#pragma unroll
    for (int kk = 0; kk < 2; ++kk)
#pragma unroll
      for (int o = 0; o < 2; ++o) { const int row = rh * 64 + ((o ^ dh ^ 1) << 5) + r;
        aB[0][kk][o] = opaque(row * 128 + (((4 * kk + g) ^ ((row >> 1) & 7)) << 4)); aB[1][kk][o] = opaque(aB[0][kk][o] + 65536); }
#pragma unroll
    for (int o = 0; o < 2; ++o)
#pragma unroll
      for (int i = 0; i < 4; ++i) aK[o][i] = opaque(L_KP + ((rh * 2 + (o ^ dh ^ 1)) * 32 + r) * 256 + (g & 1) * 8 + (((dh * 8 + 2 * i + (g >> 1)) ^ r) << 4));
    const unsigned aXw = opaque(L_X + w * 4096 + lane * 16), aXr = opaque(L_X + (w ^ 1) * 4096 + lane * 16);
    const unsigned aFw = opaque(L_M + w * 4), aFr = opaque(L_M + (w ^ 1) * 4);

    f4v acc[4][4];
    s4 Al[4][2], Ah[4][2]; s8 B[2][4][2];
    auto rdA = [&](auto tile, auto cc, auto ii) { constexpr int c = cc.value, kk = ii.value >> 1;
      constexpr unsigned so = t_slot(tile.value); constexpr int hi = so >= 65536; constexpr unsigned o = so - hi * 65536 + kk * 8192;
      if constexpr (ii.value & 1) Ah[c][kk] = tr(aA[hi][c] + o + 1024); else Al[c][kk] = tr(aA[hi][c] + o); };
    auto rdB = [&](auto tile, auto bb, auto kk) { constexpr int b = bb.value;
      constexpr unsigned so = t_slot(tile.value); constexpr int hi = so >= 65536; constexpr unsigned o = so - hi * 65536;
      B[tile.value & 1][b][kk.value] = rd16(aB[hi][kk.value][b >> 1] + o + (b & 1) * 2048); };
    auto rdNext = [&](auto tile, auto ii) { constexpr int i = ii.value;
      if constexpr (i < 2) rdB(tile, IC<0>{}, ii); else if constexpr (i < 6) rdA(tile, IC<0>{}, IC<i - 2>{});
      else rdB(tile, IC<(i - 4) / 2>{}, IC<i & 1>{}); };
    auto mm1 = [&](auto tile, auto cc, auto bb, auto kc) { constexpr int t = tile.value, c = cc.value, b = bb.value, kk = kc.value;
      const s8 av = cat(Al[c][kk], Ah[c][kk]), bv = B[t & 1][b][kk];
      SB();
      if constexpr (t == 0 && kk == 0) mma0(acc[c][b], av, bv);
      else if constexpr (kk == 0 && c == 0 && b == 0) mma(acc[c][b], av, bv, cat(Al[0][1], Ah[0][1]), B[t & 1][1][1]);
      else if constexpr (kk == 0 && c == 0 && b == 2) mma(acc[c][b], av, bv, B[t & 1][3][1]);
      else if constexpr (kk == 0 && c == 1 && b == 0) mma(acc[c][b], av, bv, cat(Al[1][1], Ah[1][1]));
      else if constexpr (kk == 0 && c == 2 && b == 0) mma(acc[c][b], av, bv, cat(Al[2][1], Ah[2][1]), cat(Al[3][0], Ah[3][0]), cat(Al[3][1], Ah[3][1]));
      else mma(acc[c][b], av, bv);
      SB(); };
    *(volatile LAS unsigned*)(size_t)aFw = 0u;
    barl();
    static_for<0, 12>([&](auto ii) { rdNext(IC<0>{}, ii); });
    static_for<0, 16>([&](auto t) {
      static_for<0, 16>([&](auto mm) { constexpr int m = mm.value;
        mm1(t, IC<(m >> 3)>{}, IC<((m >> 1) & 3)>{}, IC<(m & 1)>{});
        if constexpr (m < 12) rdA(t, IC<1 + (m >> 2)>{}, IC<(m & 3)>{}); });
      barl();
      if constexpr (t.value < 15) static_for<0, 16>([&](auto mm) { constexpr int m = mm.value;
        mm1(t, IC<2 + (m >> 3)>{}, IC<((m >> 1) & 3)>{}, IC<(m & 1)>{});
        if constexpr (m < 12) rdNext(IC<t.value + 1>{}, mm); });
    });
    s4 Kh[2][2][2][2];
    unsigned q[4][2][4];
    f4v St[2][2][2];
    auto rdK = [&](auto ii) { constexpr int i = ii.value, o = i >> 3, kb = (i >> 2) & 1, s = (i >> 1) & 1, u = i & 1;
      Kh[o][kb][s][u] = rd8(aK[o][s * 2 + u] + kb * 4096); };
    auto cvq = [&](auto bb, auto cc) { constexpr int b = bb.value, c = cc.value;
      q[b][c >> 1][(c & 1) * 2] = opaque(pk(acc[c][b][0], acc[c][b][1])); q[b][c >> 1][(c & 1) * 2 + 1] = opaque(pk(acc[c][b][2], acc[c][b][3])); };
    static_for<0, 16>([&](auto mm) { constexpr int m = mm.value;
      constexpr int cb[16] = {0, 0, 1, 1, 0, 0, 2, 2, 3, 3, 1, 1, 9, 2, 2, 9}, cc[16] = {0, 1, 0, 1, 2, 3, 0, 1, 0, 1, 2, 3, 0, 2, 3, 0};
      mm1(IC<15>{}, IC<2 + ((m >> 1) & 1)>{}, IC<(m >> 2)>{}, IC<(m & 1)>{});
      rdK(mm);
      if constexpr (cb[m] < 4) cvq(IC<cb[m] & 3>{}, IC<cc[m]>{}); });
    static_for<0, 16>([&](auto mm) { constexpr int m = mm.value, o = m >> 3, qb = (m >> 2) & 1, kb = (m >> 1) & 1, s = m & 1;
      const v4u qq = {q[2 * o + qb][s][0], q[2 * o + qb][s][1], q[2 * o + qb][s][2], q[2 * o + qb][s][3]};
      SB();
      if constexpr (s == 0) mma0(St[o][kb][qb], cat(Kh[o][kb][s][0], Kh[o][kb][s][1]), __builtin_bit_cast(s8, qq));
      else mma(St[o][kb][qb], cat(Kh[o][kb][s][0], Kh[o][kb][s][1]), __builtin_bit_cast(s8, qq));
      SB();
      if constexpr (m == 4 || m == 5) cvq(IC<3>{}, IC<(m - 2) & 3>{});
      if constexpr (m == 9 || m == 10 || m == 12 || m == 14) { constexpr int t = m == 9 ? 0 : m == 10 ? 1 : m == 12 ? 2 : 3;
        *(LAS f4v*)(size_t)(aXw + t * 1024) = St[0][t & 1][t >> 1]; } });
    SB();
    asm volatile("s_waitcnt lgkmcnt(0)" ::: "memory");
    *(volatile LAS unsigned*)(size_t)aFw = 1u;
    while (__builtin_amdgcn_readfirstlane(*(volatile LAS unsigned*)(size_t)aFr) == 0u) {}
    asm volatile("s_nop 3" : "+v"(St[1][0][0]), "+v"(St[1][0][1]), "+v"(St[1][1][0]), "+v"(St[1][1][1]) :: "memory");
    f4v xr[4];
#pragma unroll
    for (int t = 0; t < 4; ++t) xr[t] = *(LAS f4v*)(size_t)(aXr + t * 1024);
    s8 Vf[2][4];
    auto rdV = [&](auto kb, auto s) { Vf[kb.value][s.value] = rd16(aV[s.value] + kb.value * 4096); };
    SB();
    static_for<0, 8>([&](auto i) { rdV(IC<(i.value >> 2)>{}, IC<(i.value & 3)>{}); });
    SB();
#pragma unroll
    for (int t = 0; t < 4; ++t) St[1][t & 1][t >> 1] += xr[t];
    const float cs = 0.08838834764831845f * 1.4426950408889634f;
    f4v e[2][2];
    float m2[2], s2[2];
#pragma unroll
    for (int qb = 0; qb < 2; ++qb) { const f4v u = St[1][0][qb], v = St[1][1][qb];
      m2[qb] = fmaxf(fmaxf(fmaxf(fmaxf(u[0], u[1]), u[2]), fmaxf(fmaxf(u[3], v[0]), v[1])), fmaxf(v[2], v[3])); }
    v2u t0 = __builtin_amdgcn_permlane16_swap(__float_as_uint(m2[0]), __float_as_uint(m2[1]), false, false);
    unsigned z = vmax(t0[0], t0[1]);
    t0 = __builtin_amdgcn_permlane32_swap(z, z, false, false);
    z = vmax(t0[0], t0[1]);
    t0 = __builtin_amdgcn_permlane16_swap(z, z, false, false);
#pragma unroll
    for (int qb = 0; qb < 2; ++qb) {
#pragma unroll
      for (int kb = 0; kb < 2; ++kb) {
        const f4v y = (St[1][kb][qb] - __uint_as_float(t0[qb])) * cs;
#pragma unroll
        for (int i = 0; i < 4; ++i) e[qb][kb][i] = __builtin_amdgcn_exp2f(y[i]);
      }
      const f4v sm = e[qb][0] + e[qb][1];
      s2[qb] = (sm[0] + sm[2]) + (sm[1] + sm[3]);
    }
    t0 = __builtin_amdgcn_permlane16_swap(__float_as_uint(s2[0]), __float_as_uint(s2[1]), false, false);
    z = __float_as_uint(__uint_as_float(t0[0]) + __uint_as_float(t0[1]));
    t0 = __builtin_amdgcn_permlane32_swap(z, z, false, false);
    z = __float_as_uint(__builtin_amdgcn_rcpf(__uint_as_float(t0[0]) + __uint_as_float(t0[1])));
    t0 = __builtin_amdgcn_permlane16_swap(z, z, false, false);
    const __amdgpu_buffer_rsrc_t prs = __builtin_amdgcn_make_buffer_rsrc(pbase, 0, 2048, 0x00020000);
#pragma unroll
    for (int qb = 0; qb < 2; ++qb) {
      const f4v u = e[qb][0] * __uint_as_float(t0[qb]), v = e[qb][1] * __uint_as_float(t0[qb]);
      const v4u pf = {pk(u[0], u[1]), pk(u[2], u[3]), pk(v[0], v[1]), pk(v[2], v[3])};
      __builtin_amdgcn_raw_buffer_store_b128(pf, prs, lane * 16 + qb * 1024, 0, 16);
    }

    s4 Wl[4][4], Wh[4][4];
    f4v X[2][2];
    unsigned Xb[32][2][4];
    s8 Pf[4][2];
    f4v out[2][2];
    auto rdW = [&](auto rr) { constexpr int rho = rr.value, u = rho >> 3, s = (rho >> 1) & 3;
      constexpr unsigned so = w_slot(u >> 2); constexpr int hi = so >= 65536;
      constexpr unsigned o = so - hi * 65536 + ((u >> 1) & 1) * 16384 + s * 4096 + (rho & 1) * 512;
      if constexpr (rho & 1) Wh[u & 3][s] = tr(aW[hi][u & 1] + o); else Wl[u & 3][s] = tr(aW[hi][u & 1] + o); };
    auto rdP = [&](auto pp, auto ii) { constexpr int h = 2 * pp.value + (ii.value & 1), qb = ii.value >> 1;
      Pf[h & 3][qb] = rd16(aP + ((h >> 2) & 1) * 16384 + (h & 3) * 2048 + qb * 1024); };
    auto cvt = [&](auto uu, auto ii) { constexpr int u = uu.value, i = ii.value;
      Xb[u >> 1][u & 1][i] = opaque(pk(X[u & 1][i >> 1][(i & 1) * 2], X[u & 1][i >> 1][(i & 1) * 2 + 1])); };
    auto pvm = [&](auto pp, auto nn, auto qq, auto ee) { constexpr int h = 2 * pp.value + ee.value, nb = nn.value, qb = qq.value;
      const v4u xa = {Xb[h][nb][0], Xb[h][nb][1], Xb[h][nb][2], Xb[h][nb][3]};
      SB();
      if constexpr (h == 0) mma0(out[nb][qb], __builtin_bit_cast(s8, xa), Pf[h & 3][qb]);
      else if constexpr (nb == 0 && ee.value == 0) mma(out[nb][qb], __builtin_bit_cast(s8, xa), Pf[h & 3][qb], Pf[(h + 1) & 3][qb]);
      else mma(out[nb][qb], __builtin_bit_cast(s8, xa), Pf[h & 3][qb]);
      SB(); };
    barl();
    static_for<0, 10>([&](auto i) { rdW(i); });
    static_for<0, 64>([&](auto uu) {
      constexpr int u = uu.value;
      static_for<0, 8>([&](auto ii) {
        constexpr int i = ii.value, kb = i >> 2, s = i & 3, rho = 8 * u + i + 10;
        const s8 wv = cat(Wl[u & 3][s], Wh[u & 3][s]);
        SB();
        if constexpr (i == 0) mma0(X[u & 1][kb], Vf[kb][s], wv, cat(Wl[u & 3][1], Wh[u & 3][1]));
        else if constexpr (i == 4) mma0(X[u & 1][kb], Vf[kb][s], wv);
        else if constexpr (i == 2) mma(X[u & 1][kb], Vf[kb][s], wv, cat(Wl[u & 3][3], Wh[u & 3][3]));
        else mma(X[u & 1][kb], Vf[kb][s], wv);
        SB();
        if constexpr ((u & 3) == 3 && u < 32) {
          if constexpr (i == 1) { if constexpr (u == 3) waitv<0>();
                                  barl(); }
          constexpr int lo[8] = {0, 0, 2, 4, 5, 6, 8, 9}, n[8] = {0, 2, 2, 1, 1, 2, 1, 1};
          static_for<0, n[i]>([&](auto k) { rdW(IC<8 * (u + 1) + lo[i] + k.value>{}); });
        } else if constexpr (rho < 512 && !((u & 3) == 2 && i >= 6)) rdW(IC<rho>{});
        if constexpr (i == 7) cvt(uu, IC<0>{});
        if constexpr (u >= 1 && (i == 0 || i == 3 || i == 4)) cvt(IC<(u >= 1 ? u - 1 : 0)>{}, IC<(i == 0 ? 1 : (i - 1) & 3)>{});
        if constexpr (u == 32 && ((i & 3) == 1 || (i & 3) == 2)) rdP(IC<0>{}, IC<((i >> 2) * 2 + (i & 3) - 1) & 3>{});
        if constexpr ((u & 15) == 15 && u < 63 && i >= 2) rdV(IC<((i - 2) >> 2) & 1>{}, IC<(i - 2) & 3>{});
        if constexpr ((u & 15) == 0 && u > 0 && i < 2) rdV(IC<1>{}, IC<(i + 2) & 3>{});
      });
      if constexpr (u >= 32 && u <= 60 && (u & 1) == 0)
        static_for<0, 8>([&](auto ii) { constexpr int i = ii.value, pp = ((u - 32) / 2) & 15;
          pvm(IC<pp>{}, IC<((i >> 1) & 1)>{}, IC<(i >> 2)>{}, IC<(i & 1)>{});
          if constexpr ((u & 3) == 0 && i >= 4) rdP(IC<pp + 1>{}, IC<(i & 3)>{});
          if constexpr ((u & 3) == 2) {
            if constexpr (i == 1) barl();
            if constexpr (i >= 2 && i < 6) rdP(IC<pp + 1>{}, IC<(i - 2) & 3>{});
            if constexpr (i >= 6) rdW(IC<8 * (u + 2) + i - 6>{});
          } });
    });
    static_for<0, 4>([&](auto ii) { pvm(IC<15>{}, IC<0>{}, IC<(ii.value >> 1)>{}, IC<(ii.value & 1)>{});
      if constexpr (ii.value == 0) cvt(IC<63>{}, IC<1>{}); });
    asm volatile("s_nop 3"); SB();
    cvt(IC<63>{}, IC<2>{}); cvt(IC<63>{}, IC<3>{});
    SB(); asm volatile("s_nop 1");
    static_for<0, 4>([&](auto ii) { pvm(IC<15>{}, IC<1>{}, IC<(ii.value >> 1)>{}, IC<(ii.value & 1)>{}); });
    mfma_done(out[0][0], out[0][1], out[1][0], out[1][1]);
    const __amdgpu_buffer_rsrc_t ors = __builtin_amdgcn_make_buffer_rsrc(obase, 0, 31 * 4096 + 64, 0x00020000);
#pragma unroll
    for (int qb = 0; qb < 2; ++qb) {
      const v2u s0 = __builtin_amdgcn_permlane16_swap(pk(out[0][qb][0], out[0][qb][1]), pk(out[1][qb][0], out[1][qb][1]), false, false);
      const v2u s1 = __builtin_amdgcn_permlane16_swap(pk(out[0][qb][2], out[0][qb][3]), pk(out[1][qb][2], out[1][qb][3]), false, false);
      const v4u o4 = {s0[0], s1[0], s0[1], s1[1]};
      __builtin_amdgcn_raw_buffer_store_b128(o4, ors, ooff + qb * 16 * 4096, 0, 18);
    }
    __builtin_amdgcn_endpgm();
  }
#endif
}

extern "C" void kernel_launch(void* const* d_in, const int* in_sizes, int n_in, void* d_out, int out_size, void* d_ws, size_t ws_size,
                              hipStream_t stream) {
  Args a;
  for (int i = 0; i < 10; ++i) a.in[i / 5][i % 5] = (const char*)d_in[i];
  a.out = (char*)d_out; a.ws = (char*)d_ws;
  (void)hipFuncSetAttribute((const void*)gqa_fused_kernel, hipFuncAttributeMaxDynamicSharedMemorySize, LDS_BYTES);
  gqa_fused_kernel<<<256, 512, LDS_BYTES, stream>>>(a);
}
```
